```python
import torch

def _softmax(x, axis=-1):
    return torch.softmax(x, dim=axis)
def _gelu(x):
    return torch.nn.functional.gelu(x, approximate="tanh")
def _silu(x):
    return torch.nn.functional.silu(x)
def _sigmoid(x):
    return torch.sigmoid(x)
def _layernorm(x, axis=-1, eps=1e-5):
    m = x.mean(dim=axis, keepdim=True)
    v = x.var(dim=axis, keepdim=True, unbiased=False)
    return (x - m) / torch.sqrt(v + eps)
def _scatter_add(tgt, idx, src, axis=0):
    return tgt.clone().index_add_(axis, idx, src)
def _segment_sum(src, seg, n):
    out = torch.zeros((n,) + src.shape[1:], dtype=src.dtype)
    return out.index_add_(0, seg, src)
def _pad(x, pw):
    flat = tuple(v for pair in reversed(pw) for v in pair)
    return torch.nn.functional.pad(x, flat)

kernel_name = 'standalone__sliding_block_cached__decode'

def setup_inputs():

    _w = (lambda a: (lambda t: t.to(torch.bfloat16) if t.is_floating_point() else t)(torch.as_tensor(a)))
    B, Sq, D = 1, 1, 640
    Hq, Hkv, Sk, Dh = 32, 32, 4096, 192
    assert Hq % Hkv == 0, (Hq, Hkv)
    return {
        "x": _w((lambda *s: torch.randn(*s, dtype=torch.bfloat16) * 1.6)(B, Sq, D)),
        "k_cache": _w((lambda *s: torch.randn(*s, dtype=torch.bfloat16) * 1.6)(B, Hkv, Sk, Dh)),
        "v_cache": _w((lambda *s: torch.randn(*s, dtype=torch.bfloat16) * 0.05)(B, Hkv, Sk, Dh)),
        "wq": _w((lambda *s: torch.randn(*s, dtype=torch.bfloat16) * 1.6)(D, Hq * Dh) * (D ** -0.5)),
        "wo": _w((lambda *s: torch.randn(*s, dtype=torch.bfloat16) * 0.05)(Hq * Dh, D) * ((Hq * Dh) ** -0.5)),
    }

def reference(x, k_cache, v_cache, wq, wo):
    B, Sq, D = x.shape
    Hq, Dh = 32, 192
    Hkv, Sk = k_cache.shape[1], k_cache.shape[2]
    W = min(4096, max(256, Sk // 4))
    rep = Hq // Hkv
    q = (lambda x, *d: x.permute(*d))((x @ wq).reshape(B, Sq, Hq, Dh), 0, 2, 1, 3)
    k = (lambda x, n, axis: torch.repeat_interleave(x, n, dim=axis))(k_cache[:, :, Sk - W:, :], rep, axis=1)
    v = (lambda x, n, axis: torch.repeat_interleave(x, n, dim=axis))(v_cache[:, :, Sk - W:, :], rep, axis=1)
    scores = torch.einsum("bhqd,bhkd->bhqk", q, k) * (Dh ** -0.5)
    p = _softmax(scores, axis=-1)
    o = torch.einsum("bhqk,bhkd->bhqd", p, v)
    return (lambda x, *d: x.permute(*d))(o, 0, 2, 1, 3).reshape(B, Sq, Hq * Dh) @ wo
```

```cpp
#include <hip/hip_runtime.h>
#include <hip/hip_bf16.h>
#include <stdint.h>

typedef unsigned u32x4 __attribute__((ext_vector_type(4)));
typedef float f32x4 __attribute__((ext_vector_type(4)));
typedef __bf16 bf16x2_t __attribute__((ext_vector_type(2)));

constexpr int DM = 640, NH = 32, SK = 4096, DH = 192, WIN = 1024, NS = 8;
constexpr int HD = NH * DH;

__device__ __forceinline__ float bflo(unsigned u) { return __uint_as_float(u << 16); }
__device__ __forceinline__ float bfhi(unsigned u) { return __uint_as_float(u & 0xffff0000u); }
__device__ __forceinline__ float dot2(unsigned k, unsigned q, float acc) {
  return __builtin_amdgcn_fdot2_f32_bf16(__builtin_bit_cast(bf16x2_t, k), __builtin_bit_cast(bf16x2_t, q), acc, false);
}
__device__ __forceinline__ unsigned cvtpk(float lo, float hi) {
  unsigned r; asm volatile("v_cvt_pk_bf16_f32 %0, %1, %2" : "=v"(r) : "v"(lo), "v"(hi)); return r;
}
__device__ __forceinline__ float bfround(float x) { return bflo(cvtpk(x, x)); }
template <int CTRL> __device__ __forceinline__ float dpp(float x) {
  return __builtin_bit_cast(float, __builtin_amdgcn_mov_dpp(__builtin_bit_cast(int, x), CTRL, 0xf, 0xf, true));
}
constexpr int XOR1 = 0xB1, XOR2 = 0x4E, XOR7 = 0x141, XOR8 = 0x128;
__device__ __forceinline__ u32x4 ldnt(const void* p) { return __builtin_nontemporal_load(reinterpret_cast<const u32x4*>(p)); }

__global__ void __launch_bounds__(512) k_q(const uint16_t* __restrict__ x, const uint16_t* __restrict__ wq, float* __restrict__ qpart) {
  __shared__ float red[20 * 192];
  const int h = blockIdx.x >> 3, s = blockIdx.x & 7, t = threadIdx.x;
  if (t < 480) {
    const int c = t % 24, rg = t / 24;
    u32x4 w[4]; float xv[4];
#pragma unroll
    for (int j = 0; j < 4; ++j) {
      const int i = s * 80 + rg + 20 * j;
      w[j] = ldnt(wq + (size_t)i * HD + h * DH + c * 8);
      xv[j] = bflo(x[i]);
    }
    float acc[8];
#pragma unroll
    for (int e = 0; e < 8; ++e) acc[e] = 0.f;
#pragma unroll
    for (int j = 0; j < 4; ++j)
#pragma unroll
      for (int e = 0; e < 4; ++e) { acc[2 * e] = fmaf(xv[j], bflo(w[j][e]), acc[2 * e]); acc[2 * e + 1] = fmaf(xv[j], bfhi(w[j][e]), acc[2 * e + 1]); }
    *reinterpret_cast<f32x4*>(red + rg * 192 + c * 8) = f32x4{acc[0], acc[1], acc[2], acc[3]};
    *reinterpret_cast<f32x4*>(red + rg * 192 + c * 8 + 4) = f32x4{acc[4], acc[5], acc[6], acc[7]};
  }
  __syncthreads();
  if (t < 192) {
    float a = 0.f;
#pragma unroll
    for (int r = 0; r < 20; ++r) a += red[r * 192 + t];
    qpart[s * HD + h * DH + t] = a;
  }
}

__global__ void __launch_bounds__(512) k_attn(const uint16_t* __restrict__ kc, const uint16_t* __restrict__ vc, const float* __restrict__ qpart, float* __restrict__ apart) {
  __shared__ __attribute__((aligned(16))) uint16_t qs[192];
  __shared__ float op[8 * 192];
  __shared__ float ml[16];
  const int h = blockIdx.x >> 3, s = blockIdx.x & 7, t = threadIdx.x;
  const int w = t >> 6, lane = t & 63, g = lane >> 3, c = lane & 7;
  u32x4 K[2][3], V[2][3];
  const size_t base = ((size_t)h * SK + (SK - WIN) + s * 128 + w * 16 + g) * DH + c * 8;
#pragma unroll
  for (int j = 0; j < 2; ++j)
#pragma unroll
    for (int u = 0; u < 3; ++u) K[j][u] = ldnt(kc + base + j * 8 * DH + u * 64);
#pragma unroll
  for (int j = 0; j < 2; ++j)
#pragma unroll
    for (int u = 0; u < 3; ++u) V[j][u] = ldnt(vc + base + j * 8 * DH + u * 64);
  if (t < 192) {
    float a = 0.f;
#pragma unroll
    for (int r = 0; r < 8; ++r) a += qpart[r * HD + h * DH + t];
    qs[t] = (uint16_t)(cvtpk(a, a) & 0xffffu);
  }
  __syncthreads();
  u32x4 qf[3];
#pragma unroll
  for (int u = 0; u < 3; ++u) qf[u] = *reinterpret_cast<const u32x4*>(qs + u * 64 + c * 8);
  float sc[2];
#pragma unroll
  for (int j = 0; j < 2; ++j) {
    float d = 0.f;
#pragma unroll
    for (int u = 0; u < 3; ++u)
#pragma unroll
      for (int e = 0; e < 4; ++e) d = dot2(K[j][u][e], qf[u][e], d);
    d += dpp<XOR1>(d); d += dpp<XOR2>(d); d += dpp<XOR7>(d);
    sc[j] = bfround(bfround(d) * 0.07216878364870322f);
  }
  float m = fmaxf(sc[0], sc[1]);
  m = fmaxf(m, dpp<XOR8>(m));
  { auto r = __builtin_amdgcn_permlane16_swap(__float_as_uint(m), __float_as_uint(m), false, false); m = fmaxf(__uint_as_float(r[0]), __uint_as_float(r[1])); }
  { auto r = __builtin_amdgcn_permlane32_swap(__float_as_uint(m), __float_as_uint(m), false, false); m = fmaxf(__uint_as_float(r[0]), __uint_as_float(r[1])); }
  const float p0 = __builtin_amdgcn_exp2f((sc[0] - m) * 1.4426950408889634f), p1 = __builtin_amdgcn_exp2f((sc[1] - m) * 1.4426950408889634f);
  float l = p0 + p1;
  l += dpp<XOR8>(l);
  { auto r = __builtin_amdgcn_permlane16_swap(__float_as_uint(l), __float_as_uint(l), false, false); l = __uint_as_float(r[0]) + __uint_as_float(r[1]); }
  { auto r = __builtin_amdgcn_permlane32_swap(__float_as_uint(l), __float_as_uint(l), false, false); l = __uint_as_float(r[0]) + __uint_as_float(r[1]); }
  float acc[24];
#pragma unroll
  for (int u = 0; u < 3; ++u)
#pragma unroll
    for (int e = 0; e < 4; ++e) {
      acc[u * 8 + 2 * e] = fmaf(p1, bflo(V[1][u][e]), p0 * bflo(V[0][u][e]));
      acc[u * 8 + 2 * e + 1] = fmaf(p1, bfhi(V[1][u][e]), p0 * bfhi(V[0][u][e]));
    }
  float a12[12], a6[6], a3[3];
#pragma unroll
  for (int a = 0; a < 12; ++a) { auto r = __builtin_amdgcn_permlane32_swap(__float_as_uint(acc[a]), __float_as_uint(acc[a + 12]), false, false); a12[a] = __uint_as_float(r[0]) + __uint_as_float(r[1]); }
#pragma unroll
  for (int a = 0; a < 6; ++a) { auto r = __builtin_amdgcn_permlane16_swap(__float_as_uint(a12[a]), __float_as_uint(a12[a + 6]), false, false); a6[a] = __uint_as_float(r[0]) + __uint_as_float(r[1]); }
#pragma unroll
  for (int a = 0; a < 3; ++a) { const float t1 = a6[a] + dpp<XOR8>(a6[a]), t2 = a6[a + 3] + dpp<XOR8>(a6[a + 3]); a3[a] = (lane & 8) ? t2 : t1; }
  const int ib = ((lane >> 5) & 1) * 12 + ((lane >> 4) & 1) * 6 + ((lane >> 3) & 1) * 3;
#pragma unroll
  for (int a = 0; a < 3; ++a) { const int idx = ib + a; op[w * 192 + (idx >> 3) * 64 + c * 8 + (idx & 7)] = a3[a]; }
  if (lane == 0) { ml[2 * w] = m; ml[2 * w + 1] = l; }
  __syncthreads();
  if (t < 192) {
    float M = ml[0];
#pragma unroll
    for (int r = 1; r < 8; ++r) M = fmaxf(M, ml[2 * r]);
    float L = 0.f, o = 0.f;
#pragma unroll
    for (int r = 0; r < 8; ++r) { const float f = __builtin_amdgcn_exp2f((ml[2 * r] - M) * 1.4426950408889634f); L = fmaf(f, ml[2 * r + 1], L); o = fmaf(f, op[r * 192 + t], o); }
    float* dst = apart + (h * 8 + s) * 194;
    dst[2 + t] = o;
    if (t == 0) { dst[0] = M; dst[1] = L; }
  }
}

__global__ void __launch_bounds__(512) k_wo(const uint16_t* __restrict__ wo, const float* __restrict__ apart, float* __restrict__ opart) {
  __shared__ float os[192];
  __shared__ float red[48 * 80];
  const int h = blockIdx.x >> 3, s = blockIdx.x & 7, t = threadIdx.x;
  const int c = t % 10, rg = t / 10;
  u32x4 wv[4];
  if (t < 480) {
#pragma unroll
    for (int j = 0; j < 4; ++j) wv[j] = ldnt(wo + (size_t)(h * DH + rg + 48 * j) * DM + s * 80 + c * 8);
  }
  if (t < 192) {
    const float* src = apart + h * 8 * 194;
    float M = src[0];
#pragma unroll
    for (int r = 1; r < 8; ++r) M = fmaxf(M, src[r * 194]);
    float L = 0.f, o = 0.f;
#pragma unroll
    for (int r = 0; r < 8; ++r) { const float f = __builtin_amdgcn_exp2f((src[r * 194] - M) * 1.4426950408889634f); L = fmaf(f, src[r * 194 + 1], L); o = fmaf(f, src[r * 194 + 2 + t], o); }
    os[t] = bfround(o / L);
  }
  __syncthreads();
  if (t < 480) {
    float acc[8];
#pragma unroll
    for (int e = 0; e < 8; ++e) acc[e] = 0.f;
#pragma unroll
    for (int j = 0; j < 4; ++j) { const float ov = os[rg + 48 * j];
#pragma unroll
      for (int e = 0; e < 4; ++e) { acc[2 * e] = fmaf(ov, bflo(wv[j][e]), acc[2 * e]); acc[2 * e + 1] = fmaf(ov, bfhi(wv[j][e]), acc[2 * e + 1]); } }
    *reinterpret_cast<f32x4*>(red + rg * 80 + c * 8) = f32x4{acc[0], acc[1], acc[2], acc[3]};
    *reinterpret_cast<f32x4*>(red + rg * 80 + c * 8 + 4) = f32x4{acc[4], acc[5], acc[6], acc[7]};
  }
  __syncthreads();
  if (t < 80) {
    float a = 0.f;
#pragma unroll
    for (int r = 0; r < 48; ++r) a += red[r * 80 + t];
    opart[h * DM + s * 80 + t] = a;
  }
}

__global__ void __launch_bounds__(640) k_fin(const float* __restrict__ opart, uint16_t* __restrict__ out) {
  const int t = threadIdx.x;
  float a = 0.f;
#pragma unroll
  for (int r = 0; r < 32; ++r) a += opart[r * DM + t];
  out[t] = (uint16_t)(cvtpk(a, a) & 0xffffu);
}

extern "C" void kernel_launch(void* const* d_in, const int* in_sizes, int n_in, void* d_out, int out_size, void* d_ws, size_t ws_size, hipStream_t stream) {
  const uint16_t* x = (const uint16_t*)d_in[0];
  const uint16_t* kc = (const uint16_t*)d_in[1];
  const uint16_t* vc = (const uint16_t*)d_in[2];
  const uint16_t* wq = (const uint16_t*)d_in[3];
  const uint16_t* wo = (const uint16_t*)d_in[4];
  float* qpart = (float*)d_ws;
  float* apart = qpart + 8 * HD;
  float* opart = apart + 256 * 194;
  k_q<<<256, 512, 0, stream>>>(x, wq, qpart);
  k_attn<<<256, 512, 0, stream>>>(kc, vc, qpart, apart);
  k_wo<<<256, 512, 0, stream>>>(wo, apart, opart);
  k_fin<<<1, 640, 0, stream>>>(opart, (uint16_t*)d_out);
}
```

```cpp
#include <hip/hip_runtime.h>
#include <hip/hip_bf16.h>
#include <stdint.h>

typedef unsigned u32x4 __attribute__((ext_vector_type(4)));
typedef float f32x4 __attribute__((ext_vector_type(4)));
typedef __bf16 bf16x2_t __attribute__((ext_vector_type(2)));
typedef unsigned long long u64;
typedef __attribute__((address_space(1))) u64 gu64;

constexpr int DM = 640, NH = 32, SK = 4096, DH = 192, WIN = 1024, NS = 8;
constexpr int HD = NH * DH;
constexpr unsigned SPIN_CAP = 1u << 18;

constexpr int WS_EPOCH = 0;
constexpr int WS_H1 = 64;
constexpr int WS_H2 = WS_H1 + NH * NS * 192;
constexpr int WS_H3 = WS_H2 + NH * NS * 200;

__device__ __forceinline__ float bflo(unsigned u) { return __uint_as_float(u << 16); }
__device__ __forceinline__ float bfhi(unsigned u) { return __uint_as_float(u & 0xffff0000u); }
__device__ __forceinline__ float dot2(unsigned k, unsigned q, float acc) {
  return __builtin_amdgcn_fdot2_f32_bf16(__builtin_bit_cast(bf16x2_t, k), __builtin_bit_cast(bf16x2_t, q), acc, false);
}
__device__ __forceinline__ unsigned cvtpk(float lo, float hi) {
  unsigned r; asm volatile("v_cvt_pk_bf16_f32 %0, %1, %2" : "=v"(r) : "v"(lo), "v"(hi)); return r;
}
__device__ __forceinline__ float bfround(float x) { return bflo(cvtpk(x, x)); }
template <int CTRL> __device__ __forceinline__ float dpp(float x) {
  return __builtin_bit_cast(float, __builtin_amdgcn_mov_dpp(__builtin_bit_cast(int, x), CTRL, 0xf, 0xf, true));
}
constexpr int XOR1 = 0xB1, XOR2 = 0x4E, XOR7 = 0x141, XOR8 = 0x128;
__device__ __forceinline__ u32x4 ldnt(const void* p) { return __builtin_nontemporal_load(reinterpret_cast<const u32x4*>(p)); }
__device__ __forceinline__ float x16_sum(float l) {
  { auto r = __builtin_amdgcn_permlane16_swap(__float_as_uint(l), __float_as_uint(l), false, false); l = __uint_as_float(r[0]) + __uint_as_float(r[1]); }
  { auto r = __builtin_amdgcn_permlane32_swap(__float_as_uint(l), __float_as_uint(l), false, false); l = __uint_as_float(r[0]) + __uint_as_float(r[1]); }
  return l;
}
__device__ __forceinline__ float x16_max(float m) {
  { auto r = __builtin_amdgcn_permlane16_swap(__float_as_uint(m), __float_as_uint(m), false, false); m = fmaxf(__uint_as_float(r[0]), __uint_as_float(r[1])); }
  { auto r = __builtin_amdgcn_permlane32_swap(__float_as_uint(m), __float_as_uint(m), false, false); m = fmaxf(__uint_as_float(r[0]), __uint_as_float(r[1])); }
  return m;
}
#define RLX_AGENT __ATOMIC_RELAXED, __HIP_MEMORY_SCOPE_AGENT
__device__ __forceinline__ void put_g(gu64* g, u64 tag, float v) {
  const unsigned hi = ((__float_as_uint(v) + 0x80u) & 0xffffff00u) | (unsigned)(tag >> 32);
  __hip_atomic_store(g, ((u64)hi << 32) | (unsigned)tag, RLX_AGENT);
}
__device__ __forceinline__ bool chk_g(u64 x, u64 tag) { return (x & 0xffffffffffull) == tag; }
__device__ __forceinline__ float val_g(u64 x) { return __uint_as_float((unsigned)(x >> 32) & 0xffffff00u); }

constexpr float LOG2E = 1.4426950408889634f;

__global__ void __launch_bounds__(768) fused(const uint16_t* __restrict__ x, const uint16_t* __restrict__ kc, const uint16_t* __restrict__ vc,
                                             const uint16_t* __restrict__ wq, const uint16_t* __restrict__ wo, uint16_t* __restrict__ out, u64* ws_) {
  __shared__ __attribute__((aligned(16))) float red[48 * 80];
  __shared__ __attribute__((aligned(16))) float op[8 * 192];
  __shared__ __attribute__((aligned(16))) uint16_t qs[192];
  __shared__ float os[192];
  __shared__ float ml[16];
  __shared__ u64 tagw;
  gu64* ws = (gu64*)ws_;
  const int b = blockIdx.x, jj = b >> 3;
  const int h = (b & 7) * 4 + (jj >> 3), s = jj & 7;
  const int t = threadIdx.x, w = t >> 6, lane = t & 63;
  if (w < 8) {
    const int cA = t % 24, rgA = t / 24, cD = t % 10, rgD = t / 10;
    const int g = lane >> 3, c = lane & 7;
    u32x4 wa[4], K[2][3], V[2][3], wd[4]; float xv[4];
    const bool act = t < 480;
    if (act) {
#pragma unroll
      for (int j = 0; j < 4; ++j) xv[j] = bflo(x[s * 80 + rgA + 20 * j]);
#pragma unroll
      for (int j = 0; j < 4; ++j) wa[j] = ldnt(wq + (size_t)(s * 80 + rgA + 20 * j) * HD + h * DH + cA * 8);
    }
    const size_t base = ((size_t)h * SK + (SK - WIN) + s * 128 + w * 16 + g) * DH + c * 8;
#pragma unroll
    for (int j = 0; j < 2; ++j)
#pragma unroll
      for (int u = 0; u < 3; ++u) K[j][u] = ldnt(kc + base + j * 8 * DH + u * 64);
#pragma unroll
    for (int j = 0; j < 2; ++j)
#pragma unroll
      for (int u = 0; u < 3; ++u) V[j][u] = ldnt(vc + base + j * 8 * DH + u * 64);
    if (act) {
#pragma unroll
      for (int j = 0; j < 4; ++j) wd[j] = ldnt(wo + (size_t)(h * DH + rgD + 48 * j) * DM + s * 80 + cD * 8);
    }
    if (act) {
      float acc[8];
#pragma unroll
      for (int e = 0; e < 8; ++e) acc[e] = 0.f;
#pragma unroll
      for (int j = 0; j < 4; ++j)
#pragma unroll
        for (int e = 0; e < 4; ++e) { acc[2 * e] = fmaf(xv[j], bflo(wa[j][e]), acc[2 * e]); acc[2 * e + 1] = fmaf(xv[j], bfhi(wa[j][e]), acc[2 * e + 1]); }
      *reinterpret_cast<f32x4*>(red + rgA * 192 + cA * 8) = f32x4{acc[0], acc[1], acc[2], acc[3]};
      *reinterpret_cast<f32x4*>(red + rgA * 192 + cA * 8 + 4) = f32x4{acc[4], acc[5], acc[6], acc[7]};
    }
    __syncthreads();
    __syncthreads();
    u32x4 qf[3];
#pragma unroll
    for (int u = 0; u < 3; ++u) qf[u] = *reinterpret_cast<const u32x4*>(qs + u * 64 + c * 8);
    float sc[2];
#pragma unroll
    for (int j = 0; j < 2; ++j) {
      float d = 0.f;
#pragma unroll
      for (int u = 0; u < 3; ++u)
#pragma unroll
        for (int e = 0; e < 4; ++e) d = dot2(K[j][u][e], qf[u][e], d);
      d += dpp<XOR1>(d); d += dpp<XOR2>(d); d += dpp<XOR7>(d);
      sc[j] = bfround(bfround(d) * 0.07216878364870322f);
    }
    float m = fmaxf(sc[0], sc[1]);
    m = fmaxf(m, dpp<XOR8>(m));
    m = x16_max(m);
    const float p0 = __builtin_amdgcn_exp2f((sc[0] - m) * LOG2E), p1 = __builtin_amdgcn_exp2f((sc[1] - m) * LOG2E);
    float l = p0 + p1;
    l += dpp<XOR8>(l);
    l = x16_sum(l);
    float acc[24];
#pragma unroll
    for (int u = 0; u < 3; ++u)
#pragma unroll
      for (int e = 0; e < 4; ++e) {
        acc[u * 8 + 2 * e] = fmaf(p1, bflo(V[1][u][e]), p0 * bflo(V[0][u][e]));
        acc[u * 8 + 2 * e + 1] = fmaf(p1, bfhi(V[1][u][e]), p0 * bfhi(V[0][u][e]));
      }
    float a12[12], a6[6], a3[3];
#pragma unroll
    for (int a = 0; a < 12; ++a) { auto r = __builtin_amdgcn_permlane32_swap(__float_as_uint(acc[a]), __float_as_uint(acc[a + 12]), false, false); a12[a] = __uint_as_float(r[0]) + __uint_as_float(r[1]); }
#pragma unroll
    for (int a = 0; a < 6; ++a) { auto r = __builtin_amdgcn_permlane16_swap(__float_as_uint(a12[a]), __float_as_uint(a12[a + 6]), false, false); a6[a] = __uint_as_float(r[0]) + __uint_as_float(r[1]); }
#pragma unroll
    for (int a = 0; a < 3; ++a) { const float t1 = a6[a] + dpp<XOR8>(a6[a]), t2 = a6[a + 3] + dpp<XOR8>(a6[a + 3]); a3[a] = (lane & 8) ? t2 : t1; }
    const int ib = ((lane >> 5) & 1) * 12 + ((lane >> 4) & 1) * 6 + ((lane >> 3) & 1) * 3;
#pragma unroll
    for (int a = 0; a < 3; ++a) { const int idx = ib + a; op[w * 192 + (idx >> 3) * 64 + c * 8 + (idx & 7)] = a3[a]; }
    if (lane == 0) { ml[2 * w] = m; ml[2 * w + 1] = l; }
    __syncthreads();
    __syncthreads();
    if (act) {
      float ac[8];
#pragma unroll
      for (int e = 0; e < 8; ++e) ac[e] = 0.f;
#pragma unroll
      for (int j = 0; j < 4; ++j) { const float ov = os[rgD + 48 * j];
#pragma unroll
        for (int e = 0; e < 4; ++e) { ac[2 * e] = fmaf(ov, bflo(wd[j][e]), ac[2 * e]); ac[2 * e + 1] = fmaf(ov, bfhi(wd[j][e]), ac[2 * e + 1]); } }
      *reinterpret_cast<f32x4*>(red + rgD * 80 + cD * 8) = f32x4{ac[0], ac[1], ac[2], ac[3]};
      *reinterpret_cast<f32x4*>(red + rgD * 80 + cD * 8 + 4) = f32x4{ac[4], ac[5], ac[6], ac[7]};
    }
    __syncthreads();
  } else {
    const int ct = t - 512;
    if (ct == 0) tagw = (__hip_atomic_load(ws + WS_EPOCH, RLX_AGENT) + 1ull);
    __syncthreads();
    const u64 ep1 = tagw, tag = ep1 & 0xffffffffffull;
    if (ct < 192) {
      float a = 0.f;
#pragma unroll
      for (int r = 0; r < 20; ++r) a += red[r * 192 + ct];
      put_g(ws + WS_H1 + (h * 8 + s) * 192 + ct, tag, a);
      u64 gq[8];
      for (unsigned spins = 0; spins < SPIN_CAP; ++spins) {
        bool ok = true;
#pragma unroll
        for (int r = 0; r < 8; ++r) { gq[r] = __hip_atomic_load(ws + WS_H1 + (h * 8 + r) * 192 + ct, RLX_AGENT); ok &= chk_g(gq[r], tag); }
        if (__all(ok)) break;
        __builtin_amdgcn_s_sleep(1);
      }
      float q = 0.f;
#pragma unroll
      for (int r = 0; r < 8; ++r) q += val_g(gq[r]);
      qs[ct] = (uint16_t)(cvtpk(q, q) & 0xffffu);
    }
    __syncthreads();
    __syncthreads();
    {
      float M = ml[0];
#pragma unroll
      for (int r = 1; r < 8; ++r) M = fmaxf(M, ml[2 * r]);
      float L = 0.f, o = 0.f;
#pragma unroll
      for (int r = 0; r < 8; ++r) { const float f = __builtin_amdgcn_exp2f((ml[2 * r] - M) * LOG2E); L = fmaf(f, ml[2 * r + 1], L); if (ct < 192) o = fmaf(f, op[r * 192 + ct], o); }
      gu64* dst = ws + WS_H2 + (h * 8 + s) * 200;
      if (ct < 192) put_g(dst + ct, tag, o);
      else if (ct == 192) put_g(dst + 192, tag, M);
      else if (ct == 193) put_g(dst + 193, tag, L);
    }
    if (ct < 192) {
      u64 go[8], gs;
      const int sr = (lane >> 1) & 7, sw = lane & 1;
      for (unsigned spins = 0; spins < SPIN_CAP; ++spins) {
        bool ok = true;
        gs = __hip_atomic_load(ws + WS_H2 + (h * 8 + sr) * 200 + 192 + sw, RLX_AGENT); ok &= chk_g(gs, tag);
#pragma unroll
        for (int r = 0; r < 8; ++r) { go[r] = __hip_atomic_load(ws + WS_H2 + (h * 8 + r) * 200 + ct, RLX_AGENT); ok &= chk_g(go[r], tag); }
        if (__all(ok)) break;
        __builtin_amdgcn_s_sleep(1);
      }
      const float sv = val_g(gs);
      float mr[8], lr[8];
#pragma unroll
      for (int r = 0; r < 8; ++r) { mr[r] = __shfl(sv, 2 * r); lr[r] = __shfl(sv, 2 * r + 1); }
      float M = mr[0];
#pragma unroll
      for (int r = 1; r < 8; ++r) M = fmaxf(M, mr[r]);
      float L = 0.f, o = 0.f;
#pragma unroll
      for (int r = 0; r < 8; ++r) { const float f = __builtin_amdgcn_exp2f((mr[r] - M) * LOG2E); L = fmaf(f, lr[r], L); o = fmaf(f, val_g(go[r]), o); }
      os[ct] = bfround(o / L);
    }
    __syncthreads();
    __syncthreads();
    if (ct < 80) {
      float a = 0.f;
#pragma unroll
      for (int r = 0; r < 48; ++r) a += red[r * 80 + ct];
      put_g(ws + WS_H3 + (s * 32 + h) * 80 + ct, tag, a);
    }
    if (h < 10 && w == 11) {
      const int r0 = lane >> 3, col = lane & 7;
      u64 gf[4];
      for (unsigned spins = 0; spins < SPIN_CAP; ++spins) {
        bool ok = true;
#pragma unroll
        for (int k = 0; k < 4; ++k) { gf[k] = __hip_atomic_load(ws + WS_H3 + (s * 32 + r0 + 8 * k) * 80 + h * 8 + col, RLX_AGENT); ok &= chk_g(gf[k], tag); }
        if (__all(ok)) break;
        __builtin_amdgcn_s_sleep(1);
      }
      float a = (val_g(gf[0]) + val_g(gf[1])) + (val_g(gf[2]) + val_g(gf[3]));
      a += dpp<XOR8>(a);
      a = x16_sum(a);
      if (lane < 8) out[s * 80 + h * 8 + lane] = (uint16_t)(cvtpk(a, a) & 0xffffu);
      if (b == 0 && lane == 0) __hip_atomic_store(ws + WS_EPOCH, ep1, RLX_AGENT);
    }
  }
}

extern "C" void kernel_launch(void* const* d_in, const int* in_sizes, int n_in, void* d_out, int out_size, void* d_ws, size_t ws_size, hipStream_t stream) {
  fused<<<256, 768, 0, stream>>>((const uint16_t*)d_in[0], (const uint16_t*)d_in[1], (const uint16_t*)d_in[2], (const uint16_t*)d_in[3], (const uint16_t*)d_in[4],
                                 (uint16_t*)d_out, (u64*)d_ws);
}
```

```cpp
#include <hip/hip_runtime.h>
#include <hip/hip_bf16.h>
#include <stdint.h>

typedef unsigned u32x4 __attribute__((ext_vector_type(4)));
typedef float f32x4 __attribute__((ext_vector_type(4)));
typedef __bf16 bf16x2_t __attribute__((ext_vector_type(2)));
typedef unsigned long long u64;
typedef __attribute__((address_space(1))) u64 gu64;

constexpr int DM = 640, NH = 32, SK = 4096, DH = 192, WIN = 1024, NS = 8;
constexpr int HD = NH * DH;
#define NWA 2
#ifndef NV1
#define NV1 2
#endif
constexpr unsigned SPIN_CAP = 1u << 18;

constexpr int WS_EPOCH = 0;
constexpr int WS_H1 = 64;
constexpr int WS_H2 = WS_H1 + NH * NS * 192;
constexpr int WS_H3 = WS_H2 + NH * NS * 200;

__device__ __forceinline__ float bflo(unsigned u) { return __uint_as_float(u << 16); }
__device__ __forceinline__ float bfhi(unsigned u) { return __uint_as_float(u & 0xffff0000u); }
__device__ __forceinline__ float dot2(unsigned k, unsigned q, float acc) {
  return __builtin_amdgcn_fdot2_f32_bf16(__builtin_bit_cast(bf16x2_t, k), __builtin_bit_cast(bf16x2_t, q), acc, false);
}
__device__ __forceinline__ unsigned cvtpk(float lo, float hi) {
  unsigned r; asm volatile("v_cvt_pk_bf16_f32 %0, %1, %2" : "=v"(r) : "v"(lo), "v"(hi)); return r;
}
__device__ __forceinline__ float bfround(float x) { return bflo(cvtpk(x, x)); }
template <int CTRL> __device__ __forceinline__ float dpp(float x) {
  return __builtin_bit_cast(float, __builtin_amdgcn_mov_dpp(__builtin_bit_cast(int, x), CTRL, 0xf, 0xf, true));
}
constexpr int XOR1 = 0xB1, XOR2 = 0x4E, XOR7 = 0x141, XOR8 = 0x128;
__device__ __forceinline__ u32x4 ldnt(const void* p) { return __builtin_nontemporal_load(reinterpret_cast<const u32x4*>(p)); }
__device__ __forceinline__ float x16_sum(float l) {
  { auto r = __builtin_amdgcn_permlane16_swap(__float_as_uint(l), __float_as_uint(l), false, false); l = __uint_as_float(r[0]) + __uint_as_float(r[1]); }
  { auto r = __builtin_amdgcn_permlane32_swap(__float_as_uint(l), __float_as_uint(l), false, false); l = __uint_as_float(r[0]) + __uint_as_float(r[1]); }
  return l;
}
__device__ __forceinline__ float x16_max(float m) {
  { auto r = __builtin_amdgcn_permlane16_swap(__float_as_uint(m), __float_as_uint(m), false, false); m = fmaxf(__uint_as_float(r[0]), __uint_as_float(r[1])); }
  { auto r = __builtin_amdgcn_permlane32_swap(__float_as_uint(m), __float_as_uint(m), false, false); m = fmaxf(__uint_as_float(r[0]), __uint_as_float(r[1])); }
  return m;
}
#define RLX_AGENT __ATOMIC_RELAXED, __HIP_MEMORY_SCOPE_AGENT
__device__ __forceinline__ void put_g(gu64* g, u64 tag, float v) {
  const unsigned hi = ((__float_as_uint(v) + 0x80u) & 0xffffff00u) | (unsigned)(tag >> 32);
  __hip_atomic_store(g, ((u64)hi << 32) | (unsigned)tag, RLX_AGENT);
}
__device__ __forceinline__ bool chk_g(u64 x, u64 tag) { return (x & 0xffffffffffull) == tag; }
__device__ __forceinline__ float val_g(u64 x) { return __uint_as_float((unsigned)(x >> 32) & 0xffffff00u); }

#define STAMP(i)
constexpr float LOG2E = 1.4426950408889634f;
#define SB() __builtin_amdgcn_sched_barrier(0)

__global__ void __launch_bounds__(768) fused(const uint16_t* __restrict__ x, const uint16_t* __restrict__ kc, const uint16_t* __restrict__ vc,
                                             const uint16_t* __restrict__ wq, const uint16_t* __restrict__ wo, uint16_t* __restrict__ out, u64* ws_) {
  __shared__ __attribute__((aligned(16))) float red[48 * 80 + 32 * 8];
  __shared__ __attribute__((aligned(16))) float op[8 * 192];
  __shared__ __attribute__((aligned(16))) uint16_t qs[192];
  __shared__ __attribute__((aligned(16))) float os[192];
  __shared__ __attribute__((aligned(16))) float mw[8];
  __shared__ __attribute__((aligned(16))) float lw[8];
  __shared__ u64 tagw;
  gu64* ws = (gu64*)ws_;
  const int b = blockIdx.x, jj = b >> 3;
  const int h = (b & 7) * 4 + (jj >> 3), s = jj & 7;
  const int t = threadIdx.x, w = t >> 6, lane = t & 63;
  if (w < 8) {
    if (w == 0) STAMP(0);
    const int tc = t < 480 ? t : t - 32;
    const int cA = tc % 24, rgA = tc / 24, cD = tc % 10, rgD = tc / 10;
    const int g = lane >> 3, c = lane & 7;
    u32x4 wa[4], K[2][3], V[2][3], wd[4]; uint16_t xr[4];
    const bool act = t < 480;
#pragma unroll
    for (int j = 0; j < 4; ++j) xr[j] = x[s * 80 + rgA + 20 * j];
#pragma unroll
    for (int j = 0; j < 4; ++j) wa[j] = ldnt(wq + (size_t)(s * 80 + rgA + 20 * j) * HD + h * DH + cA * 8);
    SB();
    __syncthreads();
    const size_t base = ((size_t)h * SK + (SK - WIN) + s * 128 + w * 16 + g) * DH + c * 8;
#pragma unroll
    for (int j = 0; j < 2; ++j)
#pragma unroll
      for (int u = 0; u < 3; ++u) K[j][u] = ldnt(kc + base + j * 8 * DH + u * 64);
    SB();
    {
      float acc[8];
#pragma unroll
      for (int e = 0; e < 8; ++e) acc[e] = 0.f;
#pragma unroll
      for (int j = 0; j < 4; ++j) { const float xv = bflo(xr[j]);
#pragma unroll
        for (int e = 0; e < 4; ++e) { acc[2 * e] = fmaf(xv, bflo(wa[j][e]), acc[2 * e]); acc[2 * e + 1] = fmaf(xv, bfhi(wa[j][e]), acc[2 * e + 1]); } }
      float* dst = red + (act ? rgA * 192 + cA * 8 : 48 * 80 + (t - 480) * 8);
      *reinterpret_cast<f32x4*>(dst) = f32x4{acc[0], acc[1], acc[2], acc[3]};
      *reinterpret_cast<f32x4*>(dst + 4) = f32x4{acc[4], acc[5], acc[6], acc[7]};
    }
    if (w == 0) STAMP(1);
    __syncthreads();
    __syncthreads();
#pragma unroll
    for (int i = 0; i < NV1; ++i) V[i / 3][i % 3] = ldnt(vc + base + (i / 3) * 8 * DH + (i % 3) * 64);
    SB();
    __syncthreads();
    __syncthreads();
#pragma unroll
    for (int i = NV1; i < 6; ++i) V[i / 3][i % 3] = ldnt(vc + base + (i / 3) * 8 * DH + (i % 3) * 64);
    SB();
    __syncthreads();
    if (w == 0) STAMP(2);
    u32x4 qf[3];
#pragma unroll
    for (int u = 0; u < 3; ++u) qf[u] = *reinterpret_cast<const u32x4*>(qs + u * 64 + c * 8);
    float sc[2];
#pragma unroll
    for (int j = 0; j < 2; ++j) {
      float d0 = 0.f, d1 = 0.f, d2 = 0.f;
#pragma unroll
      for (int e = 0; e < 4; ++e) { d0 = dot2(K[j][0][e], qf[0][e], d0); d1 = dot2(K[j][1][e], qf[1][e], d1); d2 = dot2(K[j][2][e], qf[2][e], d2); }
      float d = (d0 + d1) + d2;
      d += dpp<XOR1>(d); d += dpp<XOR2>(d); d += dpp<XOR7>(d);
      sc[j] = bfround(bfround(d) * 0.07216878364870322f);
    }
    float m = fmaxf(sc[0], sc[1]);
    m = fmaxf(m, dpp<XOR8>(m));
    m = x16_max(m);
    if (lane == 0) mw[w] = m;
    __syncthreads();
    {
      const f32x4 m0 = *reinterpret_cast<const f32x4*>(mw), m1 = *reinterpret_cast<const f32x4*>(mw + 4);
      m = fmaxf(fmaxf(fmaxf(m0[0], m0[1]), fmaxf(m0[2], m0[3])), fmaxf(fmaxf(m1[0], m1[1]), fmaxf(m1[2], m1[3])));
    }
    const float p0 = __builtin_amdgcn_exp2f((sc[0] - m) * LOG2E), p1 = __builtin_amdgcn_exp2f((sc[1] - m) * LOG2E);
    float l = p0 + p1;
    l += dpp<XOR8>(l);
    l = x16_sum(l);
    if (lane == 0) lw[w] = l;
    SB();
#pragma unroll
    for (int j = 0; j < NWA; ++j) wd[j] = ldnt(wo + (size_t)(h * DH + rgD + 48 * j) * DM + s * 80 + cD * 8);
    SB();
    float acc[24];
#pragma unroll
    for (int u = 0; u < 3; ++u)
#pragma unroll
      for (int e = 0; e < 4; ++e) {
        acc[u * 8 + 2 * e] = fmaf(p1, bflo(V[1][u][e]), p0 * bflo(V[0][u][e]));
        acc[u * 8 + 2 * e + 1] = fmaf(p1, bfhi(V[1][u][e]), p0 * bfhi(V[0][u][e]));
      }
    float a12[12], a6[6], a3[3];
#pragma unroll
    for (int a = 0; a < 12; ++a) { auto r = __builtin_amdgcn_permlane32_swap(__float_as_uint(acc[a]), __float_as_uint(acc[a + 12]), false, false); a12[a] = __uint_as_float(r[0]) + __uint_as_float(r[1]); }
#pragma unroll
    for (int a = 0; a < 6; ++a) { auto r = __builtin_amdgcn_permlane16_swap(__float_as_uint(a12[a]), __float_as_uint(a12[a + 6]), false, false); a6[a] = __uint_as_float(r[0]) + __uint_as_float(r[1]); }
#pragma unroll
    for (int a = 0; a < 3; ++a) { const float t1 = a6[a] + dpp<XOR8>(a6[a]), t2 = a6[a + 3] + dpp<XOR8>(a6[a + 3]); a3[a] = (lane & 8) ? t2 : t1; }
    const int ib = ((lane >> 5) & 1) * 12 + ((lane >> 4) & 1) * 6 + ((lane >> 3) & 1) * 3;
#pragma unroll
    for (int a = 0; a < 3; ++a) { const int idx = ib + a; op[w * 192 + (idx >> 3) * 64 + c * 8 + (idx & 7)] = a3[a]; }
    if (w == 0) STAMP(3);
    __syncthreads();
    __syncthreads();
#pragma unroll
    for (int j = NWA; j < 4; ++j) wd[j] = ldnt(wo + (size_t)(h * DH + rgD + 48 * j) * DM + s * 80 + cD * 8);
    SB();
    __syncthreads();
    __syncthreads();
    if (w == 0) STAMP(4);
    {
      float ac[8];
#pragma unroll
      for (int e = 0; e < 8; ++e) ac[e] = 0.f;
#pragma unroll
      for (int j = 0; j < 4; ++j) { const float ov = os[rgD + 48 * j];
#pragma unroll
        for (int e = 0; e < 4; ++e) { ac[2 * e] = fmaf(ov, bflo(wd[j][e]), ac[2 * e]); ac[2 * e + 1] = fmaf(ov, bfhi(wd[j][e]), ac[2 * e + 1]); } }
      float* dst = red + (act ? rgD * 80 + cD * 8 : 48 * 80 + (t - 480) * 8);
      *reinterpret_cast<f32x4*>(dst) = f32x4{ac[0], ac[1], ac[2], ac[3]};
      *reinterpret_cast<f32x4*>(dst + 4) = f32x4{ac[4], ac[5], ac[6], ac[7]};
    }
    if (w == 0) STAMP(5);
    __syncthreads();
  } else {
    const int ct = t - 512;
    if (ct == 0) tagw = (__hip_atomic_load(ws + WS_EPOCH, RLX_AGENT) + 1ull);
    if (w == 8) STAMP(6);
    __syncthreads();
    __syncthreads();
    const u64 ep1 = tagw, tag = ep1 & 0xffffffffffull;
    u64 gq[8];
    if (ct < 192) {
      float rv[20];
#pragma unroll
      for (int r = 0; r < 20; ++r) rv[r] = red[r * 192 + ct];
      float a0 = 0.f, a1 = 0.f, a2 = 0.f, a3 = 0.f;
#pragma unroll
      for (int r = 0; r < 20; r += 4) { a0 += rv[r]; a1 += rv[r + 1]; a2 += rv[r + 2]; a3 += rv[r + 3]; }
      put_g(ws + WS_H1 + (h * 8 + s) * 192 + ct, tag, (a0 + a1) + (a2 + a3));
      if (w == 8) STAMP(7);
    }
    __syncthreads();
    __syncthreads();
    if (ct < 192) {
#pragma unroll
      for (int r = 0; r < 8; ++r) gq[r] = __hip_atomic_load(ws + WS_H1 + (h * 8 + r) * 192 + ct, RLX_AGENT);
    }
    __syncthreads();
    if (ct < 192) {
      for (unsigned spins = 0; spins < SPIN_CAP; ++spins) {
        bool ok = true;
#pragma unroll
        for (int r = 0; r < 8; ++r) ok &= chk_g(gq[r], tag);
        if (__all(ok)) break;
        if (w == 8) STAMP(13);
#pragma unroll
        for (int r = 0; r < 8; ++r) gq[r] = __hip_atomic_load(ws + WS_H1 + (h * 8 + r) * 192 + ct, RLX_AGENT);
      }
      if (w == 8) STAMP(12);
      float q = 0.f;
#pragma unroll
      for (int r = 0; r < 8; ++r) q += val_g(gq[r]);
      qs[ct] = (uint16_t)(cvtpk(q, q) & 0xffffu);
    }
    __syncthreads();
    __syncthreads();
    __syncthreads();
    {
      gu64* dst = ws + WS_H2 + (h * 8 + s) * 200;
      if (ct < 192) {
        float ov[8];
#pragma unroll
        for (int r = 0; r < 8; ++r) ov[r] = op[r * 192 + ct];
        put_g(dst + ct, tag, ((ov[0] + ov[1]) + (ov[2] + ov[3])) + ((ov[4] + ov[5]) + (ov[6] + ov[7])));
      } else if (ct < 194) {
        const f32x4 m0 = *reinterpret_cast<const f32x4*>(mw), m1 = *reinterpret_cast<const f32x4*>(mw + 4);
        const f32x4 l0 = *reinterpret_cast<const f32x4*>(lw), l1 = *reinterpret_cast<const f32x4*>(lw + 4);
        const float M = fmaxf(fmaxf(fmaxf(m0[0], m0[1]), fmaxf(m0[2], m0[3])), fmaxf(fmaxf(m1[0], m1[1]), fmaxf(m1[2], m1[3])));
        const float L = ((l0[0] + l0[1]) + (l0[2] + l0[3])) + ((l1[0] + l1[1]) + (l1[2] + l1[3]));
        put_g(dst + ct, tag, ct == 192 ? M : L);
      }
    }
    if (w == 8) STAMP(10);
    __syncthreads();
    __syncthreads();
    if (ct < 192) {
      u64 go[8], gs;
      const int sr = (lane >> 1) & 7, sw = lane & 1;
      for (unsigned spins = 0; spins < SPIN_CAP; ++spins) {
        bool ok = true;
        gs = __hip_atomic_load(ws + WS_H2 + (h * 8 + sr) * 200 + 192 + sw, RLX_AGENT); ok &= chk_g(gs, tag);
#pragma unroll
        for (int r = 0; r < 8; ++r) { go[r] = __hip_atomic_load(ws + WS_H2 + (h * 8 + r) * 200 + ct, RLX_AGENT); ok &= chk_g(go[r], tag); }
        if (__all(ok)) break;
        __builtin_amdgcn_s_sleep(1);
      }
      if (w == 8) STAMP(11);
      const float sv = val_g(gs);
      float mr[8], lr[8];
#pragma unroll
      for (int r = 0; r < 8; ++r) { mr[r] = __builtin_bit_cast(float, __builtin_amdgcn_readlane(__builtin_bit_cast(int, sv), 2 * r)); lr[r] = __builtin_bit_cast(float, __builtin_amdgcn_readlane(__builtin_bit_cast(int, sv), 2 * r + 1)); }
      float M = mr[0];
#pragma unroll
      for (int r = 1; r < 8; ++r) M = fmaxf(M, mr[r]);
      float L = 0.f, o = 0.f;
#pragma unroll
      for (int r = 0; r < 8; ++r) { const float f = __builtin_amdgcn_exp2f((mr[r] - M) * LOG2E); L = fmaf(f, lr[r], L); o = fmaf(f, val_g(go[r]), o); }
      os[ct] = bfround(o / L);
    }
    __syncthreads();
    __syncthreads();
    if (ct < 80) {
      float rv[48];
#pragma unroll
      for (int r = 0; r < 48; ++r) rv[r] = red[r * 80 + ct];
      float a0 = 0.f, a1 = 0.f, a2 = 0.f, a3 = 0.f;
#pragma unroll
      for (int r = 0; r < 48; r += 4) { a0 += rv[r]; a1 += rv[r + 1]; a2 += rv[r + 2]; a3 += rv[r + 3]; }
      put_g(ws + WS_H3 + (s * 32 + h) * 80 + ct, tag, (a0 + a1) + (a2 + a3));
    }
    if (w == 8) STAMP(8);
    if (h < 10 && w == 11) {
      const int r0 = lane >> 3, col = lane & 7;
      u64 gf[4];
      for (unsigned spins = 0; spins < SPIN_CAP; ++spins) {
        bool ok = true;
#pragma unroll
        for (int k = 0; k < 4; ++k) { gf[k] = __hip_atomic_load(ws + WS_H3 + (s * 32 + r0 + 8 * k) * 80 + h * 8 + col, RLX_AGENT); ok &= chk_g(gf[k], tag); }
        if (__all(ok)) break;
        __builtin_amdgcn_s_sleep(1);
      }
      float a = (val_g(gf[0]) + val_g(gf[1])) + (val_g(gf[2]) + val_g(gf[3]));
      a += dpp<XOR8>(a);
      a = x16_sum(a);
      if (lane < 8) out[s * 80 + h * 8 + lane] = (uint16_t)(cvtpk(a, a) & 0xffffu);
      STAMP(9);
      if (b == 0 && lane == 0) __hip_atomic_store(ws + WS_EPOCH, ep1, RLX_AGENT);
    }
  }
}

extern "C" void kernel_launch(void* const* d_in, const int* in_sizes, int n_in, void* d_out, int out_size, void* d_ws, size_t ws_size, hipStream_t stream) {
  fused<<<256, 768, 40000, stream>>>((const uint16_t*)d_in[0], (const uint16_t*)d_in[1], (const uint16_t*)d_in[2], (const uint16_t*)d_in[3], (const uint16_t*)d_in[4],
                                 (uint16_t*)d_out, (u64*)d_ws);
}
```

```cpp
#include <hip/hip_runtime.h>
#include <hip/hip_bf16.h>
#include <stdint.h>

typedef unsigned u32x4 __attribute__((ext_vector_type(4)));
typedef float f32x4 __attribute__((ext_vector_type(4)));
typedef __bf16 bf16x2_t __attribute__((ext_vector_type(2)));
typedef unsigned long long u64;
typedef __attribute__((address_space(1))) u64 gu64;

constexpr int DM = 640, NH = 32, SK = 4096, DH = 192, WIN = 1024, NS = 8;
constexpr int HD = NH * DH;
#define CODEPF 0
#define TLBPF 0
#define NWA 4
#ifndef NV1
#define NV1 2
#endif
constexpr unsigned SPIN_CAP = 1u << 18;

constexpr int WS_EPOCH = 0;
constexpr int WS_H1 = 64;
constexpr int WS_H2 = WS_H1 + NH * NS * 192;
constexpr int WS_H3 = WS_H2 + NH * NS * 200;

__device__ __forceinline__ float bflo(unsigned u) { return __uint_as_float(u << 16); }
__device__ __forceinline__ float bfhi(unsigned u) { return __uint_as_float(u & 0xffff0000u); }
__device__ __forceinline__ float dot2(unsigned k, unsigned q, float acc) {
  return __builtin_amdgcn_fdot2_f32_bf16(__builtin_bit_cast(bf16x2_t, k), __builtin_bit_cast(bf16x2_t, q), acc, false);
}
__device__ __forceinline__ unsigned cvtpk(float lo, float hi) {
  unsigned r; asm volatile("v_cvt_pk_bf16_f32 %0, %1, %2" : "=v"(r) : "v"(lo), "v"(hi)); return r;
}
__device__ __forceinline__ float bfround(float x) { return bflo(cvtpk(x, x)); }
template <int CTRL> __device__ __forceinline__ float dpp(float x) {
  return __builtin_bit_cast(float, __builtin_amdgcn_mov_dpp(__builtin_bit_cast(int, x), CTRL, 0xf, 0xf, true));
}
constexpr int XOR1 = 0xB1, XOR2 = 0x4E, XOR7 = 0x141, XOR8 = 0x128;
__device__ __forceinline__ u32x4 ldnt(const void* p) { return __builtin_nontemporal_load(reinterpret_cast<const u32x4*>(p)); }
__device__ __forceinline__ float x16_sum(float l) {
  { auto r = __builtin_amdgcn_permlane16_swap(__float_as_uint(l), __float_as_uint(l), false, false); l = __uint_as_float(r[0]) + __uint_as_float(r[1]); }
  { auto r = __builtin_amdgcn_permlane32_swap(__float_as_uint(l), __float_as_uint(l), false, false); l = __uint_as_float(r[0]) + __uint_as_float(r[1]); }
  return l;
}
__device__ __forceinline__ float x16_max(float m) {
  { auto r = __builtin_amdgcn_permlane16_swap(__float_as_uint(m), __float_as_uint(m), false, false); m = fmaxf(__uint_as_float(r[0]), __uint_as_float(r[1])); }
  { auto r = __builtin_amdgcn_permlane32_swap(__float_as_uint(m), __float_as_uint(m), false, false); m = fmaxf(__uint_as_float(r[0]), __uint_as_float(r[1])); }
  return m;
}
#define RLX_AGENT __ATOMIC_RELAXED, __HIP_MEMORY_SCOPE_AGENT
__device__ __forceinline__ void put_g(gu64* g, u64 tag, float v) {
  const unsigned hi = ((__float_as_uint(v) + 0x80u) & 0xffffff00u) | (unsigned)(tag >> 32);
  __hip_atomic_store(g, ((u64)hi << 32) | (unsigned)tag, RLX_AGENT);
}
__device__ __forceinline__ bool chk_g(u64 x, u64 tag) { return (x & 0xffffffffffull) == tag; }
__device__ __forceinline__ float val_g(u64 x) { return __uint_as_float((unsigned)(x >> 32) & 0xffffff00u); }

#define STAMP(i)
#define STAMP_DECL
#define STAMP_DUMP
constexpr float LOG2E = 1.4426950408889634f;
#define SB() __builtin_amdgcn_sched_barrier(0)

__global__ void __launch_bounds__(768) fused(const uint16_t* __restrict__ x, const uint16_t* __restrict__ kc, const uint16_t* __restrict__ vc,
                                             const uint16_t* __restrict__ wq, const uint16_t* __restrict__ wo, uint16_t* __restrict__ out, u64* ws_) {
  __shared__ __attribute__((aligned(16))) float red[48 * 80 + 32 * 8];
  __shared__ __attribute__((aligned(16))) float op[8 * 192];
  __shared__ __attribute__((aligned(16))) uint16_t qs[192];
  __shared__ __attribute__((aligned(16))) float os[192];
  __shared__ __attribute__((aligned(16))) float mw[8];
  __shared__ __attribute__((aligned(16))) float lw[8];
  __shared__ u64 tagw;
  STAMP_DECL
  u64 pc0; asm volatile("s_getpc_b64 %0" : "=s"(pc0));
  gu64* ws = (gu64*)ws_;
  const int b = blockIdx.x, jj = b >> 3;
  const int h = (b & 7) * 4 + (jj >> 3), s = jj & 7;
  const int t = threadIdx.x, w = t >> 6, lane = t & 63;
  if (w < 8) {
    if (w == 0) STAMP(0);
    const int tc = t < 480 ? t : t - 32;
    const int cA = tc % 24, rgA = tc / 24, cD = tc % 10, rgD = tc / 10;
    const int g = lane >> 3, c = lane & 7;
    u32x4 wa[4], K[2][3], V[2][3], wd[4]; uint16_t xr[4];
    const bool act = t < 480;
#pragma unroll
    for (int j = 0; j < 4; ++j) xr[j] = x[s * 80 + rgA + 20 * j];
#pragma unroll
    for (int j = 0; j < 4; ++j) wa[j] = ldnt(wq + (size_t)(s * 80 + rgA + 20 * j) * HD + h * DH + cA * 8);
    SB();
    __syncthreads();
    const size_t base = ((size_t)h * SK + (SK - WIN) + s * 128 + w * 16 + g) * DH + c * 8;
#pragma unroll
    for (int j = 0; j < 2; ++j)
#pragma unroll
      for (int u = 0; u < 3; ++u) K[j][u] = ldnt(kc + base + j * 8 * DH + u * 64);
    SB();
    {
      float acc[8];
#pragma unroll
      for (int e = 0; e < 8; ++e) acc[e] = 0.f;
#pragma unroll
      for (int j = 0; j < 4; ++j) { const float xv = bflo(xr[j]);
#pragma unroll
        for (int e = 0; e < 4; ++e) { acc[2 * e] = fmaf(xv, bflo(wa[j][e]), acc[2 * e]); acc[2 * e + 1] = fmaf(xv, bfhi(wa[j][e]), acc[2 * e + 1]); } }
      float* dst = red + (act ? rgA * 192 + cA * 8 : 48 * 80 + (t - 480) * 8);
      *reinterpret_cast<f32x4*>(dst) = f32x4{acc[0], acc[1], acc[2], acc[3]};
      *reinterpret_cast<f32x4*>(dst + 4) = f32x4{acc[4], acc[5], acc[6], acc[7]};
    }
    if (w == 0) STAMP(1);
    __syncthreads();
    __syncthreads();
#pragma unroll
    for (int i = 0; i < NV1; ++i) V[i / 3][i % 3] = ldnt(vc + base + (i / 3) * 8 * DH + (i % 3) * 64);
    SB();
    __syncthreads();
    __syncthreads();
#pragma unroll
    for (int i = NV1; i < 6; ++i) V[i / 3][i % 3] = ldnt(vc + base + (i / 3) * 8 * DH + (i % 3) * 64);
    SB();
    if (w == 0) STAMP(16);
    if (w == 7) STAMP(17);
    if (w == 3) STAMP(18);
    __syncthreads();
    if (w == 0) STAMP(2);
    u32x4 qf[3];
#pragma unroll
    for (int u = 0; u < 3; ++u) qf[u] = *reinterpret_cast<const u32x4*>(qs + u * 64 + c * 8);
    float sc[2];
#pragma unroll
    for (int j = 0; j < 2; ++j) {
      float d0 = 0.f, d1 = 0.f, d2 = 0.f;
#pragma unroll
      for (int e = 0; e < 4; ++e) { d0 = dot2(K[j][0][e], qf[0][e], d0); d1 = dot2(K[j][1][e], qf[1][e], d1); d2 = dot2(K[j][2][e], qf[2][e], d2); }
      float d = (d0 + d1) + d2;
      d += dpp<XOR1>(d); d += dpp<XOR2>(d); d += dpp<XOR7>(d);
      sc[j] = bfround(bfround(d) * 0.07216878364870322f);
    }
    float m = fmaxf(sc[0], sc[1]);
    m = fmaxf(m, dpp<XOR8>(m));
    m = x16_max(m);
    if (lane == 0) mw[w] = m;
    __syncthreads();
    {
      const f32x4 m0 = *reinterpret_cast<const f32x4*>(mw), m1 = *reinterpret_cast<const f32x4*>(mw + 4);
      m = fmaxf(fmaxf(fmaxf(m0[0], m0[1]), fmaxf(m0[2], m0[3])), fmaxf(fmaxf(m1[0], m1[1]), fmaxf(m1[2], m1[3])));
    }
    const float p0 = __builtin_amdgcn_exp2f((sc[0] - m) * LOG2E), p1 = __builtin_amdgcn_exp2f((sc[1] - m) * LOG2E);
    float l = p0 + p1;
    l += dpp<XOR8>(l);
    l = x16_sum(l);
    if (lane == 0) lw[w] = l;
    SB();
#pragma unroll
    for (int j = 0; j < NWA; ++j) wd[j] = ldnt(wo + (size_t)(h * DH + rgD + 48 * j) * DM + s * 80 + cD * 8);
    SB();
    float acc[24];
#pragma unroll
    for (int u = 0; u < 3; ++u)
#pragma unroll
      for (int e = 0; e < 4; ++e) {
        acc[u * 8 + 2 * e] = fmaf(p1, bflo(V[1][u][e]), p0 * bflo(V[0][u][e]));
        acc[u * 8 + 2 * e + 1] = fmaf(p1, bfhi(V[1][u][e]), p0 * bfhi(V[0][u][e]));
      }
    float a12[12], a6[6], a3[3];
#pragma unroll
    for (int a = 0; a < 12; ++a) { auto r = __builtin_amdgcn_permlane32_swap(__float_as_uint(acc[a]), __float_as_uint(acc[a + 12]), false, false); a12[a] = __uint_as_float(r[0]) + __uint_as_float(r[1]); }
#pragma unroll
    for (int a = 0; a < 6; ++a) { auto r = __builtin_amdgcn_permlane16_swap(__float_as_uint(a12[a]), __float_as_uint(a12[a + 6]), false, false); a6[a] = __uint_as_float(r[0]) + __uint_as_float(r[1]); }
#pragma unroll
    for (int a = 0; a < 3; ++a) { const float t1 = a6[a] + dpp<XOR8>(a6[a]), t2 = a6[a + 3] + dpp<XOR8>(a6[a + 3]); a3[a] = (lane & 8) ? t2 : t1; }
    const int ib = ((lane >> 5) & 1) * 12 + ((lane >> 4) & 1) * 6 + ((lane >> 3) & 1) * 3;
#pragma unroll
    for (int a = 0; a < 3; ++a) { const int idx = ib + a; op[w * 192 + (idx >> 3) * 64 + c * 8 + (idx & 7)] = a3[a]; }
    if (w == 0) STAMP(3);
    if (w == 7) STAMP(21);
    if (w == 4) STAMP(22);
    __syncthreads();
    __syncthreads();
#pragma unroll
    for (int j = NWA; j < 4; ++j) wd[j] = ldnt(wo + (size_t)(h * DH + rgD + 48 * j) * DM + s * 80 + cD * 8);
    SB();
    __syncthreads();
    __syncthreads();
    if (w == 0) STAMP(4);
    {
      float ac[8];
#pragma unroll
      for (int e = 0; e < 8; ++e) ac[e] = 0.f;
#pragma unroll
      for (int j = 0; j < 4; ++j) { const float ov = os[rgD + 48 * j];
#pragma unroll
        for (int e = 0; e < 4; ++e) { ac[2 * e] = fmaf(ov, bflo(wd[j][e]), ac[2 * e]); ac[2 * e + 1] = fmaf(ov, bfhi(wd[j][e]), ac[2 * e + 1]); } }
      float* dst = red + (act ? rgD * 80 + cD * 8 : 48 * 80 + (t - 480) * 8);
      *reinterpret_cast<f32x4*>(dst) = f32x4{ac[0], ac[1], ac[2], ac[3]};
      *reinterpret_cast<f32x4*>(dst + 4) = f32x4{ac[4], ac[5], ac[6], ac[7]};
    }
    if (w == 0) STAMP(5);
    __syncthreads();
  } else {
    __builtin_amdgcn_s_setprio(3);
    const int ct = t - 512;
    unsigned pfa = 0, pfb = 0; u32x4 pfv[9];
#pragma unroll
    for (int i = 0; i < 9; ++i) pfv[i] = u32x4{0u, 0u, 0u, 0u};
    if (ct == 0) tagw = (__hip_atomic_load(ws + WS_EPOCH, RLX_AGENT) + 1ull);
    if (w == 8) STAMP(6);
    __syncthreads();
    __syncthreads();
    asm volatile("" :: "v"(pfa), "v"(pfb));
#pragma unroll
    for (int i = 0; i < 9; ++i) asm volatile("" :: "v"(pfv[i]));
    const u64 ep1 = tagw, tag = ep1 & 0xffffffffffull;
    u64 gq[8];
    if (ct < 192) {
      float rv[20];
#pragma unroll
      for (int r = 0; r < 20; ++r) rv[r] = red[r * 192 + ct];
      float a0 = 0.f, a1 = 0.f, a2 = 0.f, a3 = 0.f;
#pragma unroll
      for (int r = 0; r < 20; r += 4) { a0 += rv[r]; a1 += rv[r + 1]; a2 += rv[r + 2]; a3 += rv[r + 3]; }
      put_g(ws + WS_H1 + (h * 8 + s) * 192 + ct, tag, (a0 + a1) + (a2 + a3));
      if (w == 8) STAMP(7);
    }
    __syncthreads();
    __syncthreads();
    if (ct < 192) {
#pragma unroll
      for (int r = 0; r < 8; ++r) gq[r] = __hip_atomic_load(ws + WS_H1 + (h * 8 + r) * 192 + ct, RLX_AGENT);
    }
    __syncthreads();
    if (ct < 192) {
      for (unsigned spins = 0; spins < SPIN_CAP; ++spins) {
        bool ok = true;
#pragma unroll
        for (int r = 0; r < 8; ++r) ok &= chk_g(gq[r], tag);
        if (__all(ok)) break;
        if (w == 8) STAMP(13);
#pragma unroll
        for (int r = 0; r < 8; ++r) gq[r] = __hip_atomic_load(ws + WS_H1 + (h * 8 + r) * 192 + ct, RLX_AGENT);
      }
      if (w == 8) STAMP(12);
      if (w == 9) STAMP(14);
      if (w == 10) STAMP(15);
      float q = 0.f;
#pragma unroll
      for (int r = 0; r < 8; ++r) q += val_g(gq[r]);
      qs[ct] = (uint16_t)(cvtpk(q, q) & 0xffffu);
    }
    __syncthreads();
    __syncthreads();
    if (w == 8) STAMP(19);
    __syncthreads();
    if (w == 8) STAMP(20);
    {
      gu64* dst = ws + WS_H2 + (h * 8 + s) * 200;
      if (ct < 192) {
        float ov[8];
#pragma unroll
        for (int r = 0; r < 8; ++r) ov[r] = op[r * 192 + ct];
        put_g(dst + ct, tag, ((ov[0] + ov[1]) + (ov[2] + ov[3])) + ((ov[4] + ov[5]) + (ov[6] + ov[7])));
      } else if (ct < 194) {
        const f32x4 m0 = *reinterpret_cast<const f32x4*>(mw), m1 = *reinterpret_cast<const f32x4*>(mw + 4);
        const f32x4 l0 = *reinterpret_cast<const f32x4*>(lw), l1 = *reinterpret_cast<const f32x4*>(lw + 4);
        const float M = fmaxf(fmaxf(fmaxf(m0[0], m0[1]), fmaxf(m0[2], m0[3])), fmaxf(fmaxf(m1[0], m1[1]), fmaxf(m1[2], m1[3])));
        const float L = ((l0[0] + l0[1]) + (l0[2] + l0[3])) + ((l1[0] + l1[1]) + (l1[2] + l1[3]));
        put_g(dst + ct, tag, ct == 192 ? M : L);
      }
    }
    if (w == 8) STAMP(10);
    __syncthreads();
    __syncthreads();
    if (ct < 192) {
      u64 go[8], gs;
      const int sr = (lane >> 1) & 7, sw = lane & 1;
      for (unsigned spins = 0; spins < SPIN_CAP; ++spins) {
        bool ok = true;
        gs = __hip_atomic_load(ws + WS_H2 + (h * 8 + sr) * 200 + 192 + sw, RLX_AGENT); ok &= chk_g(gs, tag);
#pragma unroll
        for (int r = 0; r < 8; ++r) { go[r] = __hip_atomic_load(ws + WS_H2 + (h * 8 + r) * 200 + ct, RLX_AGENT); ok &= chk_g(go[r], tag); }
        if (__all(ok)) break;
        __builtin_amdgcn_s_sleep(1);
      }
      if (w == 8) STAMP(11);
      const float sv = val_g(gs);
      float mr[8], lr[8];
#pragma unroll
      for (int r = 0; r < 8; ++r) { mr[r] = __builtin_bit_cast(float, __builtin_amdgcn_readlane(__builtin_bit_cast(int, sv), 2 * r)); lr[r] = __builtin_bit_cast(float, __builtin_amdgcn_readlane(__builtin_bit_cast(int, sv), 2 * r + 1)); }
      float M = mr[0];
#pragma unroll
      for (int r = 1; r < 8; ++r) M = fmaxf(M, mr[r]);
      float L = 0.f, o = 0.f;
#pragma unroll
      for (int r = 0; r < 8; ++r) { const float f = __builtin_amdgcn_exp2f((mr[r] - M) * LOG2E); L = fmaf(f, lr[r], L); o = fmaf(f, val_g(go[r]), o); }
      os[ct] = bfround(o / L);
    }
    __syncthreads();
    __syncthreads();
    if (ct < 80) {
      float rv[48];
#pragma unroll
      for (int r = 0; r < 48; ++r) rv[r] = red[r * 80 + ct];
      float a0 = 0.f, a1 = 0.f, a2 = 0.f, a3 = 0.f;
#pragma unroll
      for (int r = 0; r < 48; r += 4) { a0 += rv[r]; a1 += rv[r + 1]; a2 += rv[r + 2]; a3 += rv[r + 3]; }
      put_g(ws + WS_H3 + (s * 32 + h) * 80 + ct, tag, (a0 + a1) + (a2 + a3));
    }
    if (w == 8) STAMP(8);
    if (h < 10 && w == 11) {
      const int r0 = lane >> 3, col = lane & 7;
      u64 gf[4];
      for (unsigned spins = 0; spins < SPIN_CAP; ++spins) {
        bool ok = true;
#pragma unroll
        for (int k = 0; k < 4; ++k) { gf[k] = __hip_atomic_load(ws + WS_H3 + (s * 32 + r0 + 8 * k) * 80 + h * 8 + col, RLX_AGENT); ok &= chk_g(gf[k], tag); }
        if (__all(ok)) break;
        __builtin_amdgcn_s_sleep(1);
      }
      float a = (val_g(gf[0]) + val_g(gf[1])) + (val_g(gf[2]) + val_g(gf[3]));
      a += dpp<XOR8>(a);
      a = x16_sum(a);
      if (lane < 8) out[s * 80 + h * 8 + lane] = (uint16_t)(cvtpk(a, a) & 0xffffu);
      STAMP(9);
      if (b == 0 && lane == 0) __hip_atomic_store(ws + WS_EPOCH, ep1, RLX_AGENT);
    }
  }
  STAMP_DUMP;
}

extern "C" void kernel_launch(void* const* d_in, const int* in_sizes, int n_in, void* d_out, int out_size, void* d_ws, size_t ws_size, hipStream_t stream) {
  fused<<<256, 768, 0, stream>>>((const uint16_t*)d_in[0], (const uint16_t*)d_in[1], (const uint16_t*)d_in[2], (const uint16_t*)d_in[3], (const uint16_t*)d_in[4],
                                 (uint16_t*)d_out, (u64*)d_ws);
}
```

```cpp
#define PROBE 0
#include <hip/hip_runtime.h>
#include <hip/hip_bf16.h>
#include <stdint.h>

typedef unsigned u32x4 __attribute__((ext_vector_type(4)));
typedef float f32x4 __attribute__((ext_vector_type(4)));
typedef __bf16 bf16x2_t __attribute__((ext_vector_type(2)));
typedef unsigned long long u64;
typedef __attribute__((address_space(1))) u64 gu64;

constexpr int DM = 640, NH = 32, SK = 4096, DH = 192, WIN = 1024, NS = 8;
constexpr int HD = NH * DH;
#define CODEPF 0
#define TLBPF 0
#define NWA 4
#ifndef NV1
#define NV1 2
#endif
constexpr unsigned SPIN_CAP = 1u << 18;

constexpr int WS_EPOCH = 0;
constexpr int WS_H1 = 64;
constexpr int WS_H2 = WS_H1 + NH * NS * 192;
constexpr int WS_H3 = WS_H2 + NH * NS * 200;

__device__ __forceinline__ float bflo(unsigned u) { return __uint_as_float(u << 16); }
__device__ __forceinline__ float bfhi(unsigned u) { return __uint_as_float(u & 0xffff0000u); }
__device__ __forceinline__ float dot2(unsigned k, unsigned q, float acc) {
  return __builtin_amdgcn_fdot2_f32_bf16(__builtin_bit_cast(bf16x2_t, k), __builtin_bit_cast(bf16x2_t, q), acc, false);
}
__device__ __forceinline__ unsigned cvtpk(float lo, float hi) {
  unsigned r; asm volatile("v_cvt_pk_bf16_f32 %0, %1, %2" : "=v"(r) : "v"(lo), "v"(hi)); return r;
}
__device__ __forceinline__ float bfround(float x) { return bflo(cvtpk(x, x)); }
template <int CTRL> __device__ __forceinline__ float dpp(float x) {
  return __builtin_bit_cast(float, __builtin_amdgcn_mov_dpp(__builtin_bit_cast(int, x), CTRL, 0xf, 0xf, true));
}
constexpr int XOR1 = 0xB1, XOR2 = 0x4E, XOR7 = 0x141, XOR8 = 0x128;
__device__ __forceinline__ u32x4 ldnt(const void* p) { return __builtin_nontemporal_load(reinterpret_cast<const u32x4*>(p)); }
__device__ __forceinline__ float x16_sum(float l) {
  { auto r = __builtin_amdgcn_permlane16_swap(__float_as_uint(l), __float_as_uint(l), false, false); l = __uint_as_float(r[0]) + __uint_as_float(r[1]); }
  { auto r = __builtin_amdgcn_permlane32_swap(__float_as_uint(l), __float_as_uint(l), false, false); l = __uint_as_float(r[0]) + __uint_as_float(r[1]); }
  return l;
}
__device__ __forceinline__ float x16_max(float m) {
  { auto r = __builtin_amdgcn_permlane16_swap(__float_as_uint(m), __float_as_uint(m), false, false); m = fmaxf(__uint_as_float(r[0]), __uint_as_float(r[1])); }
  { auto r = __builtin_amdgcn_permlane32_swap(__float_as_uint(m), __float_as_uint(m), false, false); m = fmaxf(__uint_as_float(r[0]), __uint_as_float(r[1])); }
  return m;
}
#define RLX_AGENT __ATOMIC_RELAXED, __HIP_MEMORY_SCOPE_AGENT
__device__ __forceinline__ void put_g(gu64* g, u64 tag, float v) {
  const unsigned hi = ((__float_as_uint(v) + 0x80u) & 0xffffff00u) | (unsigned)(tag >> 32);
  __hip_atomic_store(g, ((u64)hi << 32) | (unsigned)tag, RLX_AGENT);
}
__device__ __forceinline__ bool chk_g(u64 x, u64 tag) { return (x & 0xffffffffffull) == tag; }
__device__ __forceinline__ float val_g(u64 x) { return __uint_as_float((unsigned)(x >> 32) & 0xffffff00u); }

#define STAMP(i) do { if ((threadIdx.x & 63) == 0) stamps[i] = __builtin_amdgcn_s_memrealtime(); } while (0)
#define STAMP_DECL __shared__ u64 stamps[32];
#define STAMP_DUMP
constexpr float LOG2E = 1.4426950408889634f;
#define SB() __builtin_amdgcn_sched_barrier(0)

__global__ void __launch_bounds__(768) fused(const uint16_t* __restrict__ x, const uint16_t* __restrict__ kc, const uint16_t* __restrict__ vc,
                                             const uint16_t* __restrict__ wq, const uint16_t* __restrict__ wo, uint16_t* __restrict__ out, u64* ws_) {
  __shared__ __attribute__((aligned(16))) float red[48 * 80 + 32 * 8];
  __shared__ __attribute__((aligned(16))) float op[8 * 192];
  __shared__ __attribute__((aligned(16))) uint16_t qs[192];
  __shared__ __attribute__((aligned(16))) float os[192];
  __shared__ __attribute__((aligned(16))) float mw[8];
  __shared__ __attribute__((aligned(16))) float lw[8];
  __shared__ u64 tagw;
  STAMP_DECL
  u64 pc0; asm volatile("s_getpc_b64 %0" : "=s"(pc0));
  gu64* ws = (gu64*)ws_;
  const int b = blockIdx.x, jj = b >> 3;
  const int h = (b & 7) * 4 + (jj >> 3), s = jj & 7;
  const int t = threadIdx.x, w = t >> 6, lane = t & 63;
  if (w < 8) {
    if (w == 0) STAMP(0);
    const int tc = t < 480 ? t : t - 32;
    const int cA = tc % 24, rgA = tc / 24, cD = tc % 10, rgD = tc / 10;
    const int g = lane >> 3, c = lane & 7;
    u32x4 wa[4], K[2][3], V[2][3], wd[4]; uint16_t xr[4];
    const bool act = t < 480;
#pragma unroll
    for (int j = 0; j < 4; ++j) xr[j] = x[s * 80 + rgA + 20 * j];
#pragma unroll
    for (int j = 0; j < 4; ++j) wa[j] = ldnt(wq + (size_t)(s * 80 + rgA + 20 * j) * HD + h * DH + cA * 8);
    SB();
    __syncthreads();
    const size_t base = ((size_t)h * SK + (SK - WIN) + s * 128 + w * 16 + g) * DH + c * 8;
#pragma unroll
    for (int j = 0; j < 2; ++j)
#pragma unroll
      for (int u = 0; u < 3; ++u) K[j][u] = ldnt(kc + base + j * 8 * DH + u * 64);
    SB();
    {
      float acc[8];
#pragma unroll
      for (int e = 0; e < 8; ++e) acc[e] = 0.f;
#pragma unroll
      for (int j = 0; j < 4; ++j) { const float xv = bflo(xr[j]);
#pragma unroll
        for (int e = 0; e < 4; ++e) { acc[2 * e] = fmaf(xv, bflo(wa[j][e]), acc[2 * e]); acc[2 * e + 1] = fmaf(xv, bfhi(wa[j][e]), acc[2 * e + 1]); } }
      float* dst = red + (act ? rgA * 192 + cA * 8 : 48 * 80 + (t - 480) * 8);
      *reinterpret_cast<f32x4*>(dst) = f32x4{acc[0], acc[1], acc[2], acc[3]};
      *reinterpret_cast<f32x4*>(dst + 4) = f32x4{acc[4], acc[5], acc[6], acc[7]};
    }
    if (w == 0) STAMP(1);
    __syncthreads();
    __syncthreads();
#pragma unroll
    for (int i = 0; i < NV1; ++i) V[i / 3][i % 3] = ldnt(vc + base + (i / 3) * 8 * DH + (i % 3) * 64);
    SB();
    __syncthreads();
    __syncthreads();
#pragma unroll
    for (int i = NV1; i < 6; ++i) V[i / 3][i % 3] = ldnt(vc + base + (i / 3) * 8 * DH + (i % 3) * 64);
    SB();
    if (w == 0) STAMP(16);
    if (w == 7) STAMP(17);
    if (w == 3) STAMP(18);
    __syncthreads();
    if (w == 0) STAMP(2);
    u32x4 qf[3];
#pragma unroll
    for (int u = 0; u < 3; ++u) qf[u] = *reinterpret_cast<const u32x4*>(qs + u * 64 + c * 8);
    float sc[2];
#pragma unroll
    for (int j = 0; j < 2; ++j) {
      float d0 = 0.f, d1 = 0.f, d2 = 0.f;
#pragma unroll
      for (int e = 0; e < 4; ++e) { d0 = dot2(K[j][0][e], qf[0][e], d0); d1 = dot2(K[j][1][e], qf[1][e], d1); d2 = dot2(K[j][2][e], qf[2][e], d2); }
      float d = (d0 + d1) + d2;
      d += dpp<XOR1>(d); d += dpp<XOR2>(d); d += dpp<XOR7>(d);
      sc[j] = bfround(bfround(d) * 0.07216878364870322f);
    }
    float m = fmaxf(sc[0], sc[1]);
    m = fmaxf(m, dpp<XOR8>(m));
    m = x16_max(m);
    if (lane == 0) mw[w] = m;
    __syncthreads();
    {
      const f32x4 m0 = *reinterpret_cast<const f32x4*>(mw), m1 = *reinterpret_cast<const f32x4*>(mw + 4);
      m = fmaxf(fmaxf(fmaxf(m0[0], m0[1]), fmaxf(m0[2], m0[3])), fmaxf(fmaxf(m1[0], m1[1]), fmaxf(m1[2], m1[3])));
    }
    const float p0 = __builtin_amdgcn_exp2f((sc[0] - m) * LOG2E), p1 = __builtin_amdgcn_exp2f((sc[1] - m) * LOG2E);
    float l = p0 + p1;
    l += dpp<XOR8>(l);
    l = x16_sum(l);
    if (lane == 0) lw[w] = l;
    SB();
#pragma unroll
    for (int j = 0; j < NWA; ++j) wd[j] = ldnt(wo + (size_t)(h * DH + rgD + 48 * j) * DM + s * 80 + cD * 8);
    SB();
    float acc[24];
#pragma unroll
    for (int u = 0; u < 3; ++u)
#pragma unroll
      for (int e = 0; e < 4; ++e) {
        acc[u * 8 + 2 * e] = fmaf(p1, bflo(V[1][u][e]), p0 * bflo(V[0][u][e]));
        acc[u * 8 + 2 * e + 1] = fmaf(p1, bfhi(V[1][u][e]), p0 * bfhi(V[0][u][e]));
      }
    float a12[12], a6[6], a3[3];
#pragma unroll
    for (int a = 0; a < 12; ++a) { auto r = __builtin_amdgcn_permlane32_swap(__float_as_uint(acc[a]), __float_as_uint(acc[a + 12]), false, false); a12[a] = __uint_as_float(r[0]) + __uint_as_float(r[1]); }
#pragma unroll
    for (int a = 0; a < 6; ++a) { auto r = __builtin_amdgcn_permlane16_swap(__float_as_uint(a12[a]), __float_as_uint(a12[a + 6]), false, false); a6[a] = __uint_as_float(r[0]) + __uint_as_float(r[1]); }
#pragma unroll
    for (int a = 0; a < 3; ++a) { const float t1 = a6[a] + dpp<XOR8>(a6[a]), t2 = a6[a + 3] + dpp<XOR8>(a6[a + 3]); a3[a] = (lane & 8) ? t2 : t1; }
    const int ib = ((lane >> 5) & 1) * 12 + ((lane >> 4) & 1) * 6 + ((lane >> 3) & 1) * 3;
#pragma unroll
    for (int a = 0; a < 3; ++a) { const int idx = ib + a; op[w * 192 + (idx >> 3) * 64 + c * 8 + (idx & 7)] = a3[a]; }
    if (w == 0) STAMP(3);
    if (w == 7) STAMP(21);
    if (w == 4) STAMP(22);
    __syncthreads();
    __syncthreads();
#pragma unroll
    for (int j = NWA; j < 4; ++j) wd[j] = ldnt(wo + (size_t)(h * DH + rgD + 48 * j) * DM + s * 80 + cD * 8);
    SB();
    __syncthreads();
    __syncthreads();
    if (w == 0) STAMP(4);
    {
      float ac[8];
#pragma unroll
      for (int e = 0; e < 8; ++e) ac[e] = 0.f;
#pragma unroll
      for (int j = 0; j < 4; ++j) { const float ov = os[rgD + 48 * j];
#pragma unroll
        for (int e = 0; e < 4; ++e) { ac[2 * e] = fmaf(ov, bflo(wd[j][e]), ac[2 * e]); ac[2 * e + 1] = fmaf(ov, bfhi(wd[j][e]), ac[2 * e + 1]); } }
      float* dst = red + (act ? rgD * 80 + cD * 8 : 48 * 80 + (t - 480) * 8);
      *reinterpret_cast<f32x4*>(dst) = f32x4{ac[0], ac[1], ac[2], ac[3]};
      *reinterpret_cast<f32x4*>(dst + 4) = f32x4{ac[4], ac[5], ac[6], ac[7]};
    }
    if (w == 0) STAMP(5);
    __syncthreads();
  } else {
    __builtin_amdgcn_s_setprio(3);
    const int ct = t - 512;
    unsigned pfa = 0, pfb = 0; u32x4 pfv[9];
#pragma unroll
    for (int i = 0; i < 9; ++i) pfv[i] = u32x4{0u, 0u, 0u, 0u};
    if (ct == 0) tagw = (__hip_atomic_load(ws + WS_EPOCH, RLX_AGENT) + 1ull);
    if (w == 8) STAMP(6);
    __syncthreads();
    __syncthreads();
    asm volatile("" :: "v"(pfa), "v"(pfb));
#pragma unroll
    for (int i = 0; i < 9; ++i) asm volatile("" :: "v"(pfv[i]));
    const u64 ep1 = tagw, tag = ep1 & 0xffffffffffull;
    u64 gq[8];
    if (ct < 192) {
      float rv[20];
#pragma unroll
      for (int r = 0; r < 20; ++r) rv[r] = red[r * 192 + ct];
      float a0 = 0.f, a1 = 0.f, a2 = 0.f, a3 = 0.f;
#pragma unroll
      for (int r = 0; r < 20; r += 4) { a0 += rv[r]; a1 += rv[r + 1]; a2 += rv[r + 2]; a3 += rv[r + 3]; }
      put_g(ws + WS_H1 + (h * 8 + s) * 192 + ct, tag, (a0 + a1) + (a2 + a3));
      if (w == 8) STAMP(7);
    }
    __syncthreads();
    __syncthreads();
    if (ct < 192) {
#pragma unroll
      for (int r = 0; r < 8; ++r) gq[r] = __hip_atomic_load(ws + WS_H1 + (h * 8 + r) * 192 + ct, RLX_AGENT);
    }
    __syncthreads();
    if (ct < 192) {
      for (unsigned spins = 0; spins < SPIN_CAP; ++spins) {
        bool ok = true;
#pragma unroll
        for (int r = 0; r < 8; ++r) ok &= chk_g(gq[r], tag);
        if (__all(ok)) break;
        if (w == 8) STAMP(13);
#pragma unroll
        for (int r = 0; r < 8; ++r) gq[r] = __hip_atomic_load(ws + WS_H1 + (h * 8 + r) * 192 + ct, RLX_AGENT);
      }
      if (w == 8) STAMP(12);
      if (w == 9) STAMP(14);
      if (w == 10) STAMP(15);
      float q = 0.f;
#pragma unroll
      for (int r = 0; r < 8; ++r) q += val_g(gq[r]);
      qs[ct] = (uint16_t)(cvtpk(q, q) & 0xffffu);
    }
    __syncthreads();
    __syncthreads();
    if (w == 8) STAMP(19);
    __syncthreads();
    if (w == 8) STAMP(20);
    {
      gu64* dst = ws + WS_H2 + (h * 8 + s) * 200;
      if (ct < 192) {
        float ov[8];
#pragma unroll
        for (int r = 0; r < 8; ++r) ov[r] = op[r * 192 + ct];
        put_g(dst + ct, tag, ((ov[0] + ov[1]) + (ov[2] + ov[3])) + ((ov[4] + ov[5]) + (ov[6] + ov[7])));
      } else if (ct < 194) {
        const f32x4 m0 = *reinterpret_cast<const f32x4*>(mw), m1 = *reinterpret_cast<const f32x4*>(mw + 4);
        const f32x4 l0 = *reinterpret_cast<const f32x4*>(lw), l1 = *reinterpret_cast<const f32x4*>(lw + 4);
        const float M = fmaxf(fmaxf(fmaxf(m0[0], m0[1]), fmaxf(m0[2], m0[3])), fmaxf(fmaxf(m1[0], m1[1]), fmaxf(m1[2], m1[3])));
        const float L = ((l0[0] + l0[1]) + (l0[2] + l0[3])) + ((l1[0] + l1[1]) + (l1[2] + l1[3]));
        put_g(dst + ct, tag, ct == 192 ? M : L);
      }
    }
    if (w == 8) STAMP(10);
    __syncthreads();
    __syncthreads();
    if (ct < 192) {
      u64 go[8], gs;
      const int sr = (lane >> 1) & 7, sw = lane & 1;
      for (unsigned spins = 0; spins < SPIN_CAP; ++spins) {
        bool ok = true;
        gs = __hip_atomic_load(ws + WS_H2 + (h * 8 + sr) * 200 + 192 + sw, RLX_AGENT); ok &= chk_g(gs, tag);
#pragma unroll
        for (int r = 0; r < 8; ++r) { go[r] = __hip_atomic_load(ws + WS_H2 + (h * 8 + r) * 200 + ct, RLX_AGENT); ok &= chk_g(go[r], tag); }
        if (__all(ok)) break;
        __builtin_amdgcn_s_sleep(1);
      }
      if (w == 8) STAMP(11);
      const float sv = val_g(gs);
      float mr[8], lr[8];
#pragma unroll
      for (int r = 0; r < 8; ++r) { mr[r] = __builtin_bit_cast(float, __builtin_amdgcn_readlane(__builtin_bit_cast(int, sv), 2 * r)); lr[r] = __builtin_bit_cast(float, __builtin_amdgcn_readlane(__builtin_bit_cast(int, sv), 2 * r + 1)); }
      float M = mr[0];
#pragma unroll
      for (int r = 1; r < 8; ++r) M = fmaxf(M, mr[r]);
      float L = 0.f, o = 0.f;
#pragma unroll
      for (int r = 0; r < 8; ++r) { const float f = __builtin_amdgcn_exp2f((mr[r] - M) * LOG2E); L = fmaf(f, lr[r], L); o = fmaf(f, val_g(go[r]), o); }
      os[ct] = bfround(o / L);
    }
    __syncthreads();
    __syncthreads();
    if (ct < 80) {
      float rv[48];
#pragma unroll
      for (int r = 0; r < 48; ++r) rv[r] = red[r * 80 + ct];
      float a0 = 0.f, a1 = 0.f, a2 = 0.f, a3 = 0.f;
#pragma unroll
      for (int r = 0; r < 48; r += 4) { a0 += rv[r]; a1 += rv[r + 1]; a2 += rv[r + 2]; a3 += rv[r + 3]; }
      put_g(ws + WS_H3 + (s * 32 + h) * 80 + ct, tag, (a0 + a1) + (a2 + a3));
    }
    if (w == 8) STAMP(8);
    if (h < 10 && w == 11) {
      const int r0 = lane >> 3, col = lane & 7;
      u64 gf[4];
      for (unsigned spins = 0; spins < SPIN_CAP; ++spins) {
        bool ok = true;
#pragma unroll
        for (int k = 0; k < 4; ++k) { gf[k] = __hip_atomic_load(ws + WS_H3 + (s * 32 + r0 + 8 * k) * 80 + h * 8 + col, RLX_AGENT); ok &= chk_g(gf[k], tag); }
        if (__all(ok)) break;
        __builtin_amdgcn_s_sleep(1);
      }
      float a = (val_g(gf[0]) + val_g(gf[1])) + (val_g(gf[2]) + val_g(gf[3]));
      a += dpp<XOR8>(a);
      a = x16_sum(a);
      if (lane < 8) out[s * 80 + h * 8 + lane] = (uint16_t)(cvtpk(a, a) & 0xffffu);
      STAMP(9);
      if (b == 0 && lane == 0) __hip_atomic_store(ws + WS_EPOCH, ep1, RLX_AGENT);
      { const volatile u64* vs = stamps; const u64 d = vs[PROBE] - vs[0]; const u64 tgt = __builtin_amdgcn_s_memrealtime() + 4 * (d > 5000 ? 0 : d);
        for (int i = 0; i < 1000000; ++i) { if (__builtin_amdgcn_s_memrealtime() >= tgt) break; __builtin_amdgcn_s_sleep(1); } }
    }
  }
  STAMP_DUMP;
}

extern "C" void kernel_launch(void* const* d_in, const int* in_sizes, int n_in, void* d_out, int out_size, void* d_ws, size_t ws_size, hipStream_t stream) {
  fused<<<256, 768, 0, stream>>>((const uint16_t*)d_in[0], (const uint16_t*)d_in[1], (const uint16_t*)d_in[2], (const uint16_t*)d_in[3], (const uint16_t*)d_in[4],
                                 (uint16_t*)d_out, (u64*)d_ws);
}
```

```cpp
#include <hip/hip_runtime.h>
#include <hip/hip_bf16.h>
#include <stdint.h>

typedef unsigned u32x4 __attribute__((ext_vector_type(4)));
typedef float f32x4 __attribute__((ext_vector_type(4)));
typedef __bf16 bf16x2_t __attribute__((ext_vector_type(2)));
typedef unsigned long long u64;
typedef __attribute__((address_space(1))) u64 gu64;

constexpr int DM = 640, NH = 32, SK = 4096, DH = 192, WIN = 1024, NS = 8;
constexpr int HD = NH * DH;
#define CODEPF 0
#define TLBPF 0
#define NWA 4
#ifndef NV1
#define NV1 2
#endif
constexpr unsigned SPIN_CAP = 1u << 18;

constexpr int WS_EPOCH = 0;
constexpr int WS_H1 = 64;
constexpr int WS_H2 = WS_H1 + NH * NS * 192;
constexpr int WS_H3 = WS_H2 + NH * NS * 200;

__device__ __forceinline__ float bflo(unsigned u) { return __uint_as_float(u << 16); }
__device__ __forceinline__ float bfhi(unsigned u) { return __uint_as_float(u & 0xffff0000u); }
__device__ __forceinline__ float dot2(unsigned k, unsigned q, float acc) {
  return __builtin_amdgcn_fdot2_f32_bf16(__builtin_bit_cast(bf16x2_t, k), __builtin_bit_cast(bf16x2_t, q), acc, false);
}
__device__ __forceinline__ unsigned cvtpk(float lo, float hi) {
  unsigned r; asm volatile("v_cvt_pk_bf16_f32 %0, %1, %2" : "=v"(r) : "v"(lo), "v"(hi)); return r;
}
__device__ __forceinline__ float bfround(float x) { return bflo(cvtpk(x, x)); }
template <int CTRL> __device__ __forceinline__ float dpp(float x) {
  return __builtin_bit_cast(float, __builtin_amdgcn_mov_dpp(__builtin_bit_cast(int, x), CTRL, 0xf, 0xf, true));
}
constexpr int XOR1 = 0xB1, XOR2 = 0x4E, XOR7 = 0x141, XOR8 = 0x128;
__device__ __forceinline__ u32x4 ldnt(const void* p) { return __builtin_nontemporal_load(reinterpret_cast<const u32x4*>(p)); }
__device__ __forceinline__ float x16_sum(float l) {
  { auto r = __builtin_amdgcn_permlane16_swap(__float_as_uint(l), __float_as_uint(l), false, false); l = __uint_as_float(r[0]) + __uint_as_float(r[1]); }
  { auto r = __builtin_amdgcn_permlane32_swap(__float_as_uint(l), __float_as_uint(l), false, false); l = __uint_as_float(r[0]) + __uint_as_float(r[1]); }
  return l;
}
__device__ __forceinline__ float x16_max(float m) {
  { auto r = __builtin_amdgcn_permlane16_swap(__float_as_uint(m), __float_as_uint(m), false, false); m = fmaxf(__uint_as_float(r[0]), __uint_as_float(r[1])); }
  { auto r = __builtin_amdgcn_permlane32_swap(__float_as_uint(m), __float_as_uint(m), false, false); m = fmaxf(__uint_as_float(r[0]), __uint_as_float(r[1])); }
  return m;
}
#define RLX_AGENT __ATOMIC_RELAXED, __HIP_MEMORY_SCOPE_AGENT
__device__ __forceinline__ void put_g(gu64* g, u64 tag, float v) {
  const unsigned hi = ((__float_as_uint(v) + 0x80u) & 0xffffff00u) | (unsigned)(tag >> 32);
  __hip_atomic_store(g, ((u64)hi << 32) | (unsigned)tag, RLX_AGENT);
}
__device__ __forceinline__ bool chk_g(u64 x, u64 tag) { return (x & 0xffffffffffull) == tag; }
__device__ __forceinline__ float val_g(u64 x) { return __uint_as_float((unsigned)(x >> 32) & 0xffffff00u); }

#define STAMP(i)
#define STAMP_DECL
#define STAMP_DUMP
constexpr float LOG2E = 1.4426950408889634f;
#define SB() __builtin_amdgcn_sched_barrier(0)

__global__ void __launch_bounds__(768) fused(const uint16_t* __restrict__ x, const uint16_t* __restrict__ kc, const uint16_t* __restrict__ vc,
                                             const uint16_t* __restrict__ wq, const uint16_t* __restrict__ wo, uint16_t* __restrict__ out, u64* ws_) {
  __shared__ __attribute__((aligned(16))) float red[48 * 80 + 32 * 8];
  __shared__ __attribute__((aligned(16))) float op[8 * 192];
  __shared__ __attribute__((aligned(16))) uint16_t qs[192];
  __shared__ __attribute__((aligned(16))) float os[192];
  __shared__ __attribute__((aligned(16))) float mw[8];
  __shared__ __attribute__((aligned(16))) float lw[8];
  __shared__ u64 tagw;
  STAMP_DECL
  u64 pc0; asm volatile("s_getpc_b64 %0" : "=s"(pc0));
  gu64* ws = (gu64*)ws_;
  const int b = blockIdx.x, jj = b >> 3;
  const int h = (b & 7) * 4 + (jj >> 3), s = jj & 7;
  const int t = threadIdx.x, w = t >> 6, lane = t & 63;
  if (w < 8) {
    if (w == 0) STAMP(0);
    const int tc = t < 480 ? t : t - 32;
    const int cA = tc % 24, rgA = tc / 24, cD = tc % 10, rgD = tc / 10;
    const int g = lane >> 3, c = lane & 7;
    u32x4 wa[4], K[2][3], V[2][3], wd[4]; uint16_t xr[4];
    const bool act = t < 480;
#pragma unroll
    for (int j = 0; j < 4; ++j) xr[j] = x[s * 80 + rgA + 20 * j];
#pragma unroll
    for (int j = 0; j < 4; ++j) wa[j] = ldnt(wq + (size_t)(s * 80 + rgA + 20 * j) * HD + h * DH + cA * 8);
    SB();
    __syncthreads();
    const size_t base = ((size_t)h * SK + (SK - WIN) + s * 128 + w * 16 + g) * DH + c * 8;
#pragma unroll
    for (int j = 0; j < 2; ++j)
#pragma unroll
      for (int u = 0; u < 3; ++u) K[j][u] = ldnt(kc + base + j * 8 * DH + u * 64);
    SB();
    {
      float acc[8];
#pragma unroll
      for (int e = 0; e < 8; ++e) acc[e] = 0.f;
#pragma unroll
      for (int j = 0; j < 4; ++j) { const float xv = bflo(xr[j]);
#pragma unroll
        for (int e = 0; e < 4; ++e) { acc[2 * e] = fmaf(xv, bflo(wa[j][e]), acc[2 * e]); acc[2 * e + 1] = fmaf(xv, bfhi(wa[j][e]), acc[2 * e + 1]); } }
      float* dst = red + (act ? rgA * 192 + cA * 8 : 48 * 80 + (t - 480) * 8);
      *reinterpret_cast<f32x4*>(dst) = f32x4{acc[0], acc[1], acc[2], acc[3]};
      *reinterpret_cast<f32x4*>(dst + 4) = f32x4{acc[4], acc[5], acc[6], acc[7]};
    }
    if (w == 0) STAMP(1);
    __syncthreads();
    __syncthreads();
#pragma unroll
    for (int i = 0; i < NV1; ++i) V[i / 3][i % 3] = ldnt(vc + base + (i / 3) * 8 * DH + (i % 3) * 64);
    SB();
    __syncthreads();
    __syncthreads();
#pragma unroll
    for (int i = NV1; i < 6; ++i) V[i / 3][i % 3] = ldnt(vc + base + (i / 3) * 8 * DH + (i % 3) * 64);
    SB();
    if (w == 0) STAMP(16);
    if (w == 7) STAMP(17);
    if (w == 3) STAMP(18);
    __syncthreads();
    if (w == 0) STAMP(2);
    u32x4 qf[3];
#pragma unroll
    for (int u = 0; u < 3; ++u) qf[u] = *reinterpret_cast<const u32x4*>(qs + u * 64 + c * 8);
    float sc[2];
#pragma unroll
    for (int j = 0; j < 2; ++j) {
      float d0 = 0.f, d1 = 0.f, d2 = 0.f;
#pragma unroll
      for (int e = 0; e < 4; ++e) { d0 = dot2(K[j][0][e], qf[0][e], d0); d1 = dot2(K[j][1][e], qf[1][e], d1); d2 = dot2(K[j][2][e], qf[2][e], d2); }
      float d = (d0 + d1) + d2;
      d += dpp<XOR1>(d); d += dpp<XOR2>(d); d += dpp<XOR7>(d);
      sc[j] = bfround(bfround(d) * 0.07216878364870322f);
    }
    float m = fmaxf(sc[0], sc[1]);
    m = fmaxf(m, dpp<XOR8>(m));
    m = x16_max(m);
    if (lane == 0) mw[w] = m;
    __syncthreads();
    {
      const f32x4 m0 = *reinterpret_cast<const f32x4*>(mw), m1 = *reinterpret_cast<const f32x4*>(mw + 4);
      m = fmaxf(fmaxf(fmaxf(m0[0], m0[1]), fmaxf(m0[2], m0[3])), fmaxf(fmaxf(m1[0], m1[1]), fmaxf(m1[2], m1[3])));
    }
    const float p0 = __builtin_amdgcn_exp2f((sc[0] - m) * LOG2E), p1 = __builtin_amdgcn_exp2f((sc[1] - m) * LOG2E);
    float l = p0 + p1;
    l += dpp<XOR8>(l);
    l = x16_sum(l);
    if (lane == 0) lw[w] = l;
    SB();
#pragma unroll
    for (int j = 0; j < NWA; ++j) wd[j] = ldnt(wo + (size_t)(h * DH + rgD + 48 * j) * DM + s * 80 + cD * 8);
    SB();
    float acc[24];
#pragma unroll
    for (int u = 0; u < 3; ++u)
#pragma unroll
      for (int e = 0; e < 4; ++e) {
        acc[u * 8 + 2 * e] = fmaf(p1, bflo(V[1][u][e]), p0 * bflo(V[0][u][e]));
        acc[u * 8 + 2 * e + 1] = fmaf(p1, bfhi(V[1][u][e]), p0 * bfhi(V[0][u][e]));
      }
    float a12[12], a6[6], a3[3];
#pragma unroll
    for (int a = 0; a < 12; ++a) { auto r = __builtin_amdgcn_permlane32_swap(__float_as_uint(acc[a]), __float_as_uint(acc[a + 12]), false, false); a12[a] = __uint_as_float(r[0]) + __uint_as_float(r[1]); }
#pragma unroll
    for (int a = 0; a < 6; ++a) { auto r = __builtin_amdgcn_permlane16_swap(__float_as_uint(a12[a]), __float_as_uint(a12[a + 6]), false, false); a6[a] = __uint_as_float(r[0]) + __uint_as_float(r[1]); }
#pragma unroll
    for (int a = 0; a < 3; ++a) { const float t1 = a6[a] + dpp<XOR8>(a6[a]), t2 = a6[a + 3] + dpp<XOR8>(a6[a + 3]); a3[a] = (lane & 8) ? t2 : t1; }
    const int ib = ((lane >> 5) & 1) * 12 + ((lane >> 4) & 1) * 6 + ((lane >> 3) & 1) * 3;
#pragma unroll
    for (int a = 0; a < 3; ++a) { const int idx = ib + a; op[w * 192 + (idx >> 3) * 64 + c * 8 + (idx & 7)] = a3[a]; }
    if (w == 0) STAMP(3);
    if (w == 7) STAMP(21);
    if (w == 4) STAMP(22);
    __syncthreads();
    __syncthreads();
#pragma unroll
    for (int j = NWA; j < 4; ++j) wd[j] = ldnt(wo + (size_t)(h * DH + rgD + 48 * j) * DM + s * 80 + cD * 8);
    SB();
    __syncthreads();
    __syncthreads();
    if (w == 0) STAMP(4);
    {
      float ac[8];
#pragma unroll
      for (int e = 0; e < 8; ++e) ac[e] = 0.f;
#pragma unroll
      for (int j = 0; j < 4; ++j) { const float ov = os[rgD + 48 * j];
#pragma unroll
        for (int e = 0; e < 4; ++e) { ac[2 * e] = fmaf(ov, bflo(wd[j][e]), ac[2 * e]); ac[2 * e + 1] = fmaf(ov, bfhi(wd[j][e]), ac[2 * e + 1]); } }
      float* dst = red + (act ? rgD * 80 + cD * 8 : 48 * 80 + (t - 480) * 8);
      *reinterpret_cast<f32x4*>(dst) = f32x4{ac[0], ac[1], ac[2], ac[3]};
      *reinterpret_cast<f32x4*>(dst + 4) = f32x4{ac[4], ac[5], ac[6], ac[7]};
    }
    if (w == 0) STAMP(5);
    __syncthreads();
  } else {
    __builtin_amdgcn_s_setprio(3);
    const int ct = t - 512;
    if (w == 11 && b < 8) asm volatile("buffer_wbl2 sc1" ::: "memory");
    unsigned pfa = 0, pfb = 0; u32x4 pfv[9];
#pragma unroll
    for (int i = 0; i < 9; ++i) pfv[i] = u32x4{0u, 0u, 0u, 0u};
    if (ct == 0) tagw = (__hip_atomic_load(ws + WS_EPOCH, RLX_AGENT) + 1ull);
    if (w == 8) STAMP(6);
    __syncthreads();
    __syncthreads();
    asm volatile("" :: "v"(pfa), "v"(pfb));
#pragma unroll
    for (int i = 0; i < 9; ++i) asm volatile("" :: "v"(pfv[i]));
    const u64 ep1 = tagw, tag = ep1 & 0xffffffffffull;
    u64 gq[8];
    if (ct < 192) {
      float rv[20];
#pragma unroll
      for (int r = 0; r < 20; ++r) rv[r] = red[r * 192 + ct];
      float a0 = 0.f, a1 = 0.f, a2 = 0.f, a3 = 0.f;
#pragma unroll
      for (int r = 0; r < 20; r += 4) { a0 += rv[r]; a1 += rv[r + 1]; a2 += rv[r + 2]; a3 += rv[r + 3]; }
      put_g(ws + WS_H1 + (h * 8 + s) * 192 + ct, tag, (a0 + a1) + (a2 + a3));
      if (w == 8) STAMP(7);
    }
    __syncthreads();
    __syncthreads();
    if (ct < 192) {
#pragma unroll
      for (int r = 0; r < 8; ++r) gq[r] = __hip_atomic_load(ws + WS_H1 + (h * 8 + r) * 192 + ct, RLX_AGENT);
    }
    __syncthreads();
    if (ct < 192) {
      for (unsigned spins = 0; spins < SPIN_CAP; ++spins) {
        bool ok = true;
#pragma unroll
        for (int r = 0; r < 8; ++r) ok &= chk_g(gq[r], tag);
        if (__all(ok)) break;
        if (w == 8) STAMP(13);
#pragma unroll
        for (int r = 0; r < 8; ++r) gq[r] = __hip_atomic_load(ws + WS_H1 + (h * 8 + r) * 192 + ct, RLX_AGENT);
      }
      if (w == 8) STAMP(12);
      if (w == 9) STAMP(14);
      if (w == 10) STAMP(15);
      float q = 0.f;
#pragma unroll
      for (int r = 0; r < 8; ++r) q += val_g(gq[r]);
      qs[ct] = (uint16_t)(cvtpk(q, q) & 0xffffu);
    }
    __syncthreads();
    __syncthreads();
    if (w == 8) STAMP(19);
    __syncthreads();
    if (w == 8) STAMP(20);
    {
      gu64* dst = ws + WS_H2 + (h * 8 + s) * 200;
      if (ct < 192) {
        float ov[8];
#pragma unroll
        for (int r = 0; r < 8; ++r) ov[r] = op[r * 192 + ct];
        put_g(dst + ct, tag, ((ov[0] + ov[1]) + (ov[2] + ov[3])) + ((ov[4] + ov[5]) + (ov[6] + ov[7])));
      } else if (ct < 194) {
        const f32x4 m0 = *reinterpret_cast<const f32x4*>(mw), m1 = *reinterpret_cast<const f32x4*>(mw + 4);
        const f32x4 l0 = *reinterpret_cast<const f32x4*>(lw), l1 = *reinterpret_cast<const f32x4*>(lw + 4);
        const float M = fmaxf(fmaxf(fmaxf(m0[0], m0[1]), fmaxf(m0[2], m0[3])), fmaxf(fmaxf(m1[0], m1[1]), fmaxf(m1[2], m1[3])));
        const float L = ((l0[0] + l0[1]) + (l0[2] + l0[3])) + ((l1[0] + l1[1]) + (l1[2] + l1[3]));
        put_g(dst + ct, tag, ct == 192 ? M : L);
      }
    }
    if (w == 8) STAMP(10);
    __syncthreads();
    __syncthreads();
    if (ct < 192) {
      u64 go[8], gs;
      const int sr = (lane >> 1) & 7, sw = lane & 1;
      for (unsigned spins = 0; spins < SPIN_CAP; ++spins) {
        bool ok = true;
        gs = __hip_atomic_load(ws + WS_H2 + (h * 8 + sr) * 200 + 192 + sw, RLX_AGENT); ok &= chk_g(gs, tag);
#pragma unroll
        for (int r = 0; r < 8; ++r) { go[r] = __hip_atomic_load(ws + WS_H2 + (h * 8 + r) * 200 + ct, RLX_AGENT); ok &= chk_g(go[r], tag); }
        if (__all(ok)) break;
        __builtin_amdgcn_s_sleep(1);
      }
      if (w == 8) STAMP(11);
      const float sv = val_g(gs);
      float mr[8], lr[8];
#pragma unroll
      for (int r = 0; r < 8; ++r) { mr[r] = __builtin_bit_cast(float, __builtin_amdgcn_readlane(__builtin_bit_cast(int, sv), 2 * r)); lr[r] = __builtin_bit_cast(float, __builtin_amdgcn_readlane(__builtin_bit_cast(int, sv), 2 * r + 1)); }
      float M = mr[0];
#pragma unroll
      for (int r = 1; r < 8; ++r) M = fmaxf(M, mr[r]);
      float L = 0.f, o = 0.f;
#pragma unroll
      for (int r = 0; r < 8; ++r) { const float f = __builtin_amdgcn_exp2f((mr[r] - M) * LOG2E); L = fmaf(f, lr[r], L); o = fmaf(f, val_g(go[r]), o); }
      os[ct] = bfround(o / L);
    }
    __syncthreads();
    __syncthreads();
    if (ct < 80) {
      float rv[48];
#pragma unroll
      for (int r = 0; r < 48; ++r) rv[r] = red[r * 80 + ct];
      float a0 = 0.f, a1 = 0.f, a2 = 0.f, a3 = 0.f;
#pragma unroll
      for (int r = 0; r < 48; r += 4) { a0 += rv[r]; a1 += rv[r + 1]; a2 += rv[r + 2]; a3 += rv[r + 3]; }
      put_g(ws + WS_H3 + (s * 32 + h) * 80 + ct, tag, (a0 + a1) + (a2 + a3));
    }
    if (w == 8) STAMP(8);
    if (h < 10 && w == 11) {
      const int r0 = lane >> 3, col = lane & 7;
      u64 gf[4];
      for (unsigned spins = 0; spins < SPIN_CAP; ++spins) {
        bool ok = true;
#pragma unroll
        for (int k = 0; k < 4; ++k) { gf[k] = __hip_atomic_load(ws + WS_H3 + (s * 32 + r0 + 8 * k) * 80 + h * 8 + col, RLX_AGENT); ok &= chk_g(gf[k], tag); }
        if (__all(ok)) break;
        __builtin_amdgcn_s_sleep(1);
      }
      float a = (val_g(gf[0]) + val_g(gf[1])) + (val_g(gf[2]) + val_g(gf[3]));
      a += dpp<XOR8>(a);
      a = x16_sum(a);
      if (lane < 8) out[s * 80 + h * 8 + lane] = (uint16_t)(cvtpk(a, a) & 0xffffu);
      STAMP(9);
      if (b == 0 && lane == 0) __hip_atomic_store(ws + WS_EPOCH, ep1, RLX_AGENT);
    }
  }
  STAMP_DUMP;
}

extern "C" void kernel_launch(void* const* d_in, const int* in_sizes, int n_in, void* d_out, int out_size, void* d_ws, size_t ws_size, hipStream_t stream) {
  fused<<<256, 768, 0, stream>>>((const uint16_t*)d_in[0], (const uint16_t*)d_in[1], (const uint16_t*)d_in[2], (const uint16_t*)d_in[3], (const uint16_t*)d_in[4],
                                 (uint16_t*)d_out, (u64*)d_ws);
}
```

```cpp
#define PROBE 2
#include <hip/hip_runtime.h>
#include <hip/hip_bf16.h>
#include <stdint.h>

typedef unsigned u32x4 __attribute__((ext_vector_type(4)));
typedef float f32x4 __attribute__((ext_vector_type(4)));
typedef __bf16 bf16x2_t __attribute__((ext_vector_type(2)));
typedef unsigned long long u64;
typedef __attribute__((address_space(1))) u64 gu64;

constexpr int DM = 640, NH = 32, SK = 4096, DH = 192, WIN = 1024, NS = 8;
constexpr int HD = NH * DH;
#define CODEPF 0
#define TLBPF 0
#define NWA 4
#ifndef NV1
#define NV1 2
#endif
constexpr unsigned SPIN_CAP = 1u << 18;

constexpr int WS_EPOCH = 0;
constexpr int WS_H1 = 64;
constexpr int WS_H2 = WS_H1 + NH * NS * 192;
constexpr int WS_H3 = WS_H2 + NH * NS * 200;

__device__ __forceinline__ float bflo(unsigned u) { return __uint_as_float(u << 16); }
__device__ __forceinline__ float bfhi(unsigned u) { return __uint_as_float(u & 0xffff0000u); }
__device__ __forceinline__ float dot2(unsigned k, unsigned q, float acc) {
  return __builtin_amdgcn_fdot2_f32_bf16(__builtin_bit_cast(bf16x2_t, k), __builtin_bit_cast(bf16x2_t, q), acc, false);
}
__device__ __forceinline__ unsigned cvtpk(float lo, float hi) {
  unsigned r; asm volatile("v_cvt_pk_bf16_f32 %0, %1, %2" : "=v"(r) : "v"(lo), "v"(hi)); return r;
}
__device__ __forceinline__ float bfround(float x) { return bflo(cvtpk(x, x)); }
template <int CTRL> __device__ __forceinline__ float dpp(float x) {
  return __builtin_bit_cast(float, __builtin_amdgcn_mov_dpp(__builtin_bit_cast(int, x), CTRL, 0xf, 0xf, true));
}
constexpr int XOR1 = 0xB1, XOR2 = 0x4E, XOR7 = 0x141, XOR8 = 0x128;
__device__ __forceinline__ u32x4 ldnt(const void* p) { return __builtin_nontemporal_load(reinterpret_cast<const u32x4*>(p)); }
__device__ __forceinline__ float x16_sum(float l) {
  { auto r = __builtin_amdgcn_permlane16_swap(__float_as_uint(l), __float_as_uint(l), false, false); l = __uint_as_float(r[0]) + __uint_as_float(r[1]); }
  { auto r = __builtin_amdgcn_permlane32_swap(__float_as_uint(l), __float_as_uint(l), false, false); l = __uint_as_float(r[0]) + __uint_as_float(r[1]); }
  return l;
}
__device__ __forceinline__ float x16_max(float m) {
  { auto r = __builtin_amdgcn_permlane16_swap(__float_as_uint(m), __float_as_uint(m), false, false); m = fmaxf(__uint_as_float(r[0]), __uint_as_float(r[1])); }
  { auto r = __builtin_amdgcn_permlane32_swap(__float_as_uint(m), __float_as_uint(m), false, false); m = fmaxf(__uint_as_float(r[0]), __uint_as_float(r[1])); }
  return m;
}
#define RLX_AGENT __ATOMIC_RELAXED, __HIP_MEMORY_SCOPE_AGENT
__device__ __forceinline__ void put_g(gu64* g, u64 tag, float v) {
  const unsigned hi = ((__float_as_uint(v) + 0x80u) & 0xffffff00u) | (unsigned)(tag >> 32);
  __hip_atomic_store(g, ((u64)hi << 32) | (unsigned)tag, RLX_AGENT);
}
__device__ __forceinline__ bool chk_g(u64 x, u64 tag) { return (x & 0xffffffffffull) == tag; }
__device__ __forceinline__ float val_g(u64 x) { return __uint_as_float((unsigned)(x >> 32) & 0xffffff00u); }

#define STAMP(i) do { if ((threadIdx.x & 63) == 0) stamps[i] = __builtin_amdgcn_s_memrealtime(); } while (0)
#define STAMP_DECL __shared__ u64 stamps[32];
#define STAMP_DUMP
constexpr float LOG2E = 1.4426950408889634f;
#define SB() __builtin_amdgcn_sched_barrier(0)

__global__ void __launch_bounds__(768) fused(const uint16_t* __restrict__ x, const uint16_t* __restrict__ kc, const uint16_t* __restrict__ vc,
                                             const uint16_t* __restrict__ wq, const uint16_t* __restrict__ wo, uint16_t* __restrict__ out, u64* ws_) {
  __shared__ __attribute__((aligned(16))) float red[48 * 80 + 32 * 8];
  __shared__ __attribute__((aligned(16))) float op[8 * 192];
  __shared__ __attribute__((aligned(16))) uint16_t qs[192];
  __shared__ __attribute__((aligned(16))) float os[192];
  __shared__ __attribute__((aligned(16))) float mw[8];
  __shared__ __attribute__((aligned(16))) float lw[8];
  __shared__ u64 tagw;
  STAMP_DECL
  u64 pc0; asm volatile("s_getpc_b64 %0" : "=s"(pc0));
  gu64* ws = (gu64*)ws_;
  const int b = blockIdx.x, jj = b >> 3;
  const int h = (b & 7) * 4 + (jj >> 3), s = jj & 7;
  const int t = threadIdx.x, w = t >> 6, lane = t & 63;
  if (w < 8) {
    if (w == 0) STAMP(0);
    const int tc = t < 480 ? t : t - 32;
    const int cA = tc % 24, rgA = tc / 24, cD = tc % 10, rgD = tc / 10;
    const int g = lane >> 3, c = lane & 7;
    u32x4 wa[4], K[2][3], V[2][3], wd[4]; uint16_t xr[4];
    const bool act = t < 480;
#pragma unroll
    for (int j = 0; j < 4; ++j) xr[j] = x[s * 80 + rgA + 20 * j];
#pragma unroll
    for (int j = 0; j < 4; ++j) wa[j] = ldnt(wq + (size_t)(s * 80 + rgA + 20 * j) * HD + h * DH + cA * 8);
    SB();
    __syncthreads();
    const size_t base = ((size_t)h * SK + (SK - WIN) + s * 128 + w * 16 + g) * DH + c * 8;
#pragma unroll
    for (int j = 0; j < 2; ++j)
#pragma unroll
      for (int u = 0; u < 3; ++u) K[j][u] = ldnt(kc + base + j * 8 * DH + u * 64);
    SB();
    {
      float acc[8];
#pragma unroll
      for (int e = 0; e < 8; ++e) acc[e] = 0.f;
#pragma unroll
      for (int j = 0; j < 4; ++j) { const float xv = bflo(xr[j]);
#pragma unroll
        for (int e = 0; e < 4; ++e) { acc[2 * e] = fmaf(xv, bflo(wa[j][e]), acc[2 * e]); acc[2 * e + 1] = fmaf(xv, bfhi(wa[j][e]), acc[2 * e + 1]); } }
      float* dst = red + (act ? rgA * 192 + cA * 8 : 48 * 80 + (t - 480) * 8);
      *reinterpret_cast<f32x4*>(dst) = f32x4{acc[0], acc[1], acc[2], acc[3]};
      *reinterpret_cast<f32x4*>(dst + 4) = f32x4{acc[4], acc[5], acc[6], acc[7]};
    }
    if (w == 0) STAMP(1);
    __syncthreads();
    __syncthreads();
#pragma unroll
    for (int i = 0; i < NV1; ++i) V[i / 3][i % 3] = ldnt(vc + base + (i / 3) * 8 * DH + (i % 3) * 64);
    SB();
    __syncthreads();
    __syncthreads();
#pragma unroll
    for (int i = NV1; i < 6; ++i) V[i / 3][i % 3] = ldnt(vc + base + (i / 3) * 8 * DH + (i % 3) * 64);
    SB();
    if (w == 0) STAMP(16);
    if (w == 7) STAMP(17);
    if (w == 3) STAMP(18);
    __syncthreads();
    if (w == 0) STAMP(2);
    u32x4 qf[3];
#pragma unroll
    for (int u = 0; u < 3; ++u) qf[u] = *reinterpret_cast<const u32x4*>(qs + u * 64 + c * 8);
    float sc[2];
#pragma unroll
    for (int j = 0; j < 2; ++j) {
      float d0 = 0.f, d1 = 0.f, d2 = 0.f;
#pragma unroll
      for (int e = 0; e < 4; ++e) { d0 = dot2(K[j][0][e], qf[0][e], d0); d1 = dot2(K[j][1][e], qf[1][e], d1); d2 = dot2(K[j][2][e], qf[2][e], d2); }
      float d = (d0 + d1) + d2;
      d += dpp<XOR1>(d); d += dpp<XOR2>(d); d += dpp<XOR7>(d);
      sc[j] = bfround(bfround(d) * 0.07216878364870322f);
    }
    float m = fmaxf(sc[0], sc[1]);
    m = fmaxf(m, dpp<XOR8>(m));
    m = x16_max(m);
    if (lane == 0) mw[w] = m;
    __syncthreads();
    {
      const f32x4 m0 = *reinterpret_cast<const f32x4*>(mw), m1 = *reinterpret_cast<const f32x4*>(mw + 4);
      m = fmaxf(fmaxf(fmaxf(m0[0], m0[1]), fmaxf(m0[2], m0[3])), fmaxf(fmaxf(m1[0], m1[1]), fmaxf(m1[2], m1[3])));
    }
    const float p0 = __builtin_amdgcn_exp2f((sc[0] - m) * LOG2E), p1 = __builtin_amdgcn_exp2f((sc[1] - m) * LOG2E);
    float l = p0 + p1;
    l += dpp<XOR8>(l);
    l = x16_sum(l);
    if (lane == 0) lw[w] = l;
    SB();
#pragma unroll
    for (int j = 0; j < NWA; ++j) wd[j] = ldnt(wo + (size_t)(h * DH + rgD + 48 * j) * DM + s * 80 + cD * 8);
    SB();
    float acc[24];
#pragma unroll
    for (int u = 0; u < 3; ++u)
#pragma unroll
      for (int e = 0; e < 4; ++e) {
        acc[u * 8 + 2 * e] = fmaf(p1, bflo(V[1][u][e]), p0 * bflo(V[0][u][e]));
        acc[u * 8 + 2 * e + 1] = fmaf(p1, bfhi(V[1][u][e]), p0 * bfhi(V[0][u][e]));
      }
    float a12[12], a6[6], a3[3];
#pragma unroll
    for (int a = 0; a < 12; ++a) { auto r = __builtin_amdgcn_permlane32_swap(__float_as_uint(acc[a]), __float_as_uint(acc[a + 12]), false, false); a12[a] = __uint_as_float(r[0]) + __uint_as_float(r[1]); }
#pragma unroll
    for (int a = 0; a < 6; ++a) { auto r = __builtin_amdgcn_permlane16_swap(__float_as_uint(a12[a]), __float_as_uint(a12[a + 6]), false, false); a6[a] = __uint_as_float(r[0]) + __uint_as_float(r[1]); }
#pragma unroll
    for (int a = 0; a < 3; ++a) { const float t1 = a6[a] + dpp<XOR8>(a6[a]), t2 = a6[a + 3] + dpp<XOR8>(a6[a + 3]); a3[a] = (lane & 8) ? t2 : t1; }
    const int ib = ((lane >> 5) & 1) * 12 + ((lane >> 4) & 1) * 6 + ((lane >> 3) & 1) * 3;
#pragma unroll
    for (int a = 0; a < 3; ++a) { const int idx = ib + a; op[w * 192 + (idx >> 3) * 64 + c * 8 + (idx & 7)] = a3[a]; }
    if (w == 0) STAMP(3);
    if (w == 7) STAMP(21);
    if (w == 4) STAMP(22);
    __syncthreads();
    __syncthreads();
#pragma unroll
    for (int j = NWA; j < 4; ++j) wd[j] = ldnt(wo + (size_t)(h * DH + rgD + 48 * j) * DM + s * 80 + cD * 8);
    SB();
    __syncthreads();
    __syncthreads();
    if (w == 0) STAMP(4);
    {
      float ac[8];
#pragma unroll
      for (int e = 0; e < 8; ++e) ac[e] = 0.f;
#pragma unroll
      for (int j = 0; j < 4; ++j) { const float ov = os[rgD + 48 * j];
#pragma unroll
        for (int e = 0; e < 4; ++e) { ac[2 * e] = fmaf(ov, bflo(wd[j][e]), ac[2 * e]); ac[2 * e + 1] = fmaf(ov, bfhi(wd[j][e]), ac[2 * e + 1]); } }
      float* dst = red + (act ? rgD * 80 + cD * 8 : 48 * 80 + (t - 480) * 8);
      *reinterpret_cast<f32x4*>(dst) = f32x4{ac[0], ac[1], ac[2], ac[3]};
      *reinterpret_cast<f32x4*>(dst + 4) = f32x4{ac[4], ac[5], ac[6], ac[7]};
    }
    if (w == 0) STAMP(5);
    __syncthreads();
  } else {
    __builtin_amdgcn_s_setprio(3);
    const int ct = t - 512;
    unsigned pfa = 0, pfb = 0; u32x4 pfv[9];
#pragma unroll
    for (int i = 0; i < 9; ++i) pfv[i] = u32x4{0u, 0u, 0u, 0u};
    if (ct == 0) tagw = (__hip_atomic_load(ws + WS_EPOCH, RLX_AGENT) + 1ull);
    if (w == 8) STAMP(6);
    __syncthreads();
    __syncthreads();
    asm volatile("" :: "v"(pfa), "v"(pfb));
#pragma unroll
    for (int i = 0; i < 9; ++i) asm volatile("" :: "v"(pfv[i]));
    const u64 ep1 = tagw, tag = ep1 & 0xffffffffffull;
    u64 gq[8];
    if (ct < 192) {
      float rv[20];
#pragma unroll
      for (int r = 0; r < 20; ++r) rv[r] = red[r * 192 + ct];
      float a0 = 0.f, a1 = 0.f, a2 = 0.f, a3 = 0.f;
#pragma unroll
      for (int r = 0; r < 20; r += 4) { a0 += rv[r]; a1 += rv[r + 1]; a2 += rv[r + 2]; a3 += rv[r + 3]; }
      put_g(ws + WS_H1 + (h * 8 + s) * 192 + ct, tag, (a0 + a1) + (a2 + a3));
      if (w == 8) STAMP(7);
    }
    __syncthreads();
    __syncthreads();
    if (ct < 192) {
#pragma unroll
      for (int r = 0; r < 8; ++r) gq[r] = __hip_atomic_load(ws + WS_H1 + (h * 8 + r) * 192 + ct, RLX_AGENT);
    }
    __syncthreads();
    if (ct < 192) {
      for (unsigned spins = 0; spins < SPIN_CAP; ++spins) {
        bool ok = true;
#pragma unroll
        for (int r = 0; r < 8; ++r) ok &= chk_g(gq[r], tag);
        if (__all(ok)) break;
        if (w == 8) STAMP(13);
#pragma unroll
        for (int r = 0; r < 8; ++r) gq[r] = __hip_atomic_load(ws + WS_H1 + (h * 8 + r) * 192 + ct, RLX_AGENT);
      }
      if (w == 8) STAMP(12);
      if (w == 9) STAMP(14);
      if (w == 10) STAMP(15);
      float q = 0.f;
#pragma unroll
      for (int r = 0; r < 8; ++r) q += val_g(gq[r]);
      qs[ct] = (uint16_t)(cvtpk(q, q) & 0xffffu);
    }
    __syncthreads();
    __syncthreads();
    if (w == 8) STAMP(19);
    __syncthreads();
    if (w == 8) STAMP(20);
    {
      gu64* dst = ws + WS_H2 + (h * 8 + s) * 200;
      if (ct < 192) {
        float ov[8];
#pragma unroll
        for (int r = 0; r < 8; ++r) ov[r] = op[r * 192 + ct];
        put_g(dst + ct, tag, ((ov[0] + ov[1]) + (ov[2] + ov[3])) + ((ov[4] + ov[5]) + (ov[6] + ov[7])));
      } else if (ct < 194) {
        const f32x4 m0 = *reinterpret_cast<const f32x4*>(mw), m1 = *reinterpret_cast<const f32x4*>(mw + 4);
        const f32x4 l0 = *reinterpret_cast<const f32x4*>(lw), l1 = *reinterpret_cast<const f32x4*>(lw + 4);
        const float M = fmaxf(fmaxf(fmaxf(m0[0], m0[1]), fmaxf(m0[2], m0[3])), fmaxf(fmaxf(m1[0], m1[1]), fmaxf(m1[2], m1[3])));
        const float L = ((l0[0] + l0[1]) + (l0[2] + l0[3])) + ((l1[0] + l1[1]) + (l1[2] + l1[3]));
        put_g(dst + ct, tag, ct == 192 ? M : L);
      }
    }
    if (w == 8) STAMP(10);
    __syncthreads();
    __syncthreads();
    if (ct < 192) {
      u64 go[8], gs;
      const int sr = (lane >> 1) & 7, sw = lane & 1;
      for (unsigned spins = 0; spins < SPIN_CAP; ++spins) {
        bool ok = true;
        gs = __hip_atomic_load(ws + WS_H2 + (h * 8 + sr) * 200 + 192 + sw, RLX_AGENT); ok &= chk_g(gs, tag);
#pragma unroll
        for (int r = 0; r < 8; ++r) { go[r] = __hip_atomic_load(ws + WS_H2 + (h * 8 + r) * 200 + ct, RLX_AGENT); ok &= chk_g(go[r], tag); }
        if (__all(ok)) break;
        __builtin_amdgcn_s_sleep(1);
      }
      if (w == 8) STAMP(11);
      const float sv = val_g(gs);
      float mr[8], lr[8];
#pragma unroll
      for (int r = 0; r < 8; ++r) { mr[r] = __builtin_bit_cast(float, __builtin_amdgcn_readlane(__builtin_bit_cast(int, sv), 2 * r)); lr[r] = __builtin_bit_cast(float, __builtin_amdgcn_readlane(__builtin_bit_cast(int, sv), 2 * r + 1)); }
      float M = mr[0];
#pragma unroll
      for (int r = 1; r < 8; ++r) M = fmaxf(M, mr[r]);
      float L = 0.f, o = 0.f;
#pragma unroll
      for (int r = 0; r < 8; ++r) { const float f = __builtin_amdgcn_exp2f((mr[r] - M) * LOG2E); L = fmaf(f, lr[r], L); o = fmaf(f, val_g(go[r]), o); }
      os[ct] = bfround(o / L);
    }
    __syncthreads();
    __syncthreads();
    if (ct < 80) {
      float rv[48];
#pragma unroll
      for (int r = 0; r < 48; ++r) rv[r] = red[r * 80 + ct];
      float a0 = 0.f, a1 = 0.f, a2 = 0.f, a3 = 0.f;
#pragma unroll
      for (int r = 0; r < 48; r += 4) { a0 += rv[r]; a1 += rv[r + 1]; a2 += rv[r + 2]; a3 += rv[r + 3]; }
      put_g(ws + WS_H3 + (s * 32 + h) * 80 + ct, tag, (a0 + a1) + (a2 + a3));
    }
    if (w == 8) STAMP(8);
    if (h < 10 && w == 11) {
      const int r0 = lane >> 3, col = lane & 7;
      u64 gf[4];
      for (unsigned spins = 0; spins < SPIN_CAP; ++spins) {
        bool ok = true;
#pragma unroll
        for (int k = 0; k < 4; ++k) { gf[k] = __hip_atomic_load(ws + WS_H3 + (s * 32 + r0 + 8 * k) * 80 + h * 8 + col, RLX_AGENT); ok &= chk_g(gf[k], tag); }
        if (__all(ok)) break;
        __builtin_amdgcn_s_sleep(1);
      }
      float a = (val_g(gf[0]) + val_g(gf[1])) + (val_g(gf[2]) + val_g(gf[3]));
      a += dpp<XOR8>(a);
      a = x16_sum(a);
      if (lane < 8) out[s * 80 + h * 8 + lane] = (uint16_t)(cvtpk(a, a) & 0xffffu);
      STAMP(9);
      if (b == 0 && lane == 0) __hip_atomic_store(ws + WS_EPOCH, ep1, RLX_AGENT);
      { const volatile u64* vs = stamps; const u64 d = vs[PROBE] - vs[0]; const u64 tgt = __builtin_amdgcn_s_memrealtime() + 4 * (d > 5000 ? 0 : d);
        for (int i = 0; i < 1000000; ++i) { if (__builtin_amdgcn_s_memrealtime() >= tgt) break; __builtin_amdgcn_s_sleep(1); } }
    }
  }
  STAMP_DUMP;
}

extern "C" void kernel_launch(void* const* d_in, const int* in_sizes, int n_in, void* d_out, int out_size, void* d_ws, size_t ws_size, hipStream_t stream) {
  fused<<<256, 768, 0, stream>>>((const uint16_t*)d_in[0], (const uint16_t*)d_in[1], (const uint16_t*)d_in[2], (const uint16_t*)d_in[3], (const uint16_t*)d_in[4],
                                 (uint16_t*)d_out, (u64*)d_ws);
}
```

```cpp
#define PROBE 0
#include <hip/hip_runtime.h>
#include <hip/hip_bf16.h>
#include <stdint.h>

typedef unsigned u32x4 __attribute__((ext_vector_type(4)));
typedef float f32x4 __attribute__((ext_vector_type(4)));
typedef __bf16 bf16x2_t __attribute__((ext_vector_type(2)));
typedef unsigned long long u64;
typedef __attribute__((address_space(1))) u64 gu64;

constexpr int DM = 640, NH = 32, SK = 4096, DH = 192, WIN = 1024, NS = 8;
constexpr int HD = NH * DH;
#define CODEPF 16
#define TLBPF 0
#define NWA 4
#ifndef NV1
#define NV1 2
#endif
constexpr unsigned SPIN_CAP = 1u << 18;

constexpr int WS_EPOCH = 0;
constexpr int WS_H1 = 64;
constexpr int WS_H2 = WS_H1 + NH * NS * 192;
constexpr int WS_H3 = WS_H2 + NH * NS * 200;

__device__ __forceinline__ float bflo(unsigned u) { return __uint_as_float(u << 16); }
__device__ __forceinline__ float bfhi(unsigned u) { return __uint_as_float(u & 0xffff0000u); }
__device__ __forceinline__ float dot2(unsigned k, unsigned q, float acc) {
  return __builtin_amdgcn_fdot2_f32_bf16(__builtin_bit_cast(bf16x2_t, k), __builtin_bit_cast(bf16x2_t, q), acc, false);
}
__device__ __forceinline__ unsigned cvtpk(float lo, float hi) {
  unsigned r; asm volatile("v_cvt_pk_bf16_f32 %0, %1, %2" : "=v"(r) : "v"(lo), "v"(hi)); return r;
}
__device__ __forceinline__ float bfround(float x) { return bflo(cvtpk(x, x)); }
template <int CTRL> __device__ __forceinline__ float dpp(float x) {
  return __builtin_bit_cast(float, __builtin_amdgcn_mov_dpp(__builtin_bit_cast(int, x), CTRL, 0xf, 0xf, true));
}
constexpr int XOR1 = 0xB1, XOR2 = 0x4E, XOR7 = 0x141, XOR8 = 0x128;
__device__ __forceinline__ u32x4 ldnt(const void* p) { return __builtin_nontemporal_load(reinterpret_cast<const u32x4*>(p)); }
__device__ __forceinline__ float x16_sum(float l) {
  { auto r = __builtin_amdgcn_permlane16_swap(__float_as_uint(l), __float_as_uint(l), false, false); l = __uint_as_float(r[0]) + __uint_as_float(r[1]); }
  { auto r = __builtin_amdgcn_permlane32_swap(__float_as_uint(l), __float_as_uint(l), false, false); l = __uint_as_float(r[0]) + __uint_as_float(r[1]); }
  return l;
}
__device__ __forceinline__ float x16_max(float m) {
  { auto r = __builtin_amdgcn_permlane16_swap(__float_as_uint(m), __float_as_uint(m), false, false); m = fmaxf(__uint_as_float(r[0]), __uint_as_float(r[1])); }
  { auto r = __builtin_amdgcn_permlane32_swap(__float_as_uint(m), __float_as_uint(m), false, false); m = fmaxf(__uint_as_float(r[0]), __uint_as_float(r[1])); }
  return m;
}
#define RLX_AGENT __ATOMIC_RELAXED, __HIP_MEMORY_SCOPE_AGENT
__device__ __forceinline__ void put_g(gu64* g, u64 tag, float v) {
  const unsigned hi = ((__float_as_uint(v) + 0x80u) & 0xffffff00u) | (unsigned)(tag >> 32);
  __hip_atomic_store(g, ((u64)hi << 32) | (unsigned)tag, RLX_AGENT);
}
__device__ __forceinline__ bool chk_g(u64 x, u64 tag) { return (x & 0xffffffffffull) == tag; }
__device__ __forceinline__ float val_g(u64 x) { return __uint_as_float((unsigned)(x >> 32) & 0xffffff00u); }

#define STAMP(i) do { if ((threadIdx.x & 63) == 0) stamps[i] = __builtin_amdgcn_s_memrealtime(); } while (0)
#define STAMP_DECL __shared__ u64 stamps[32];
#define STAMP_DUMP
constexpr float LOG2E = 1.4426950408889634f;
#define SB() __builtin_amdgcn_sched_barrier(0)

__global__ void __launch_bounds__(768) fused(const uint16_t* __restrict__ x, const uint16_t* __restrict__ kc, const uint16_t* __restrict__ vc,
                                             const uint16_t* __restrict__ wq, const uint16_t* __restrict__ wo, uint16_t* __restrict__ out, u64* ws_) {
  __shared__ __attribute__((aligned(16))) float red[48 * 80 + 32 * 8];
  __shared__ __attribute__((aligned(16))) float op[8 * 192];
  __shared__ __attribute__((aligned(16))) uint16_t qs[192];
  __shared__ __attribute__((aligned(16))) float os[192];
  __shared__ __attribute__((aligned(16))) float mw[8];
  __shared__ __attribute__((aligned(16))) float lw[8];
  __shared__ u64 tagw;
  STAMP_DECL
  u64 pc0; asm volatile("s_getpc_b64 %0" : "=s"(pc0));
  gu64* ws = (gu64*)ws_;
  const int b = blockIdx.x, jj = b >> 3;
  const int h = (b & 7) * 4 + (jj >> 3), s = jj & 7;
  const int t = threadIdx.x, w = t >> 6, lane = t & 63;
  if (w < 8) {
    if (w == 0) STAMP(0);
    const int tc = t < 480 ? t : t - 32;
    const int cA = tc % 24, rgA = tc / 24, cD = tc % 10, rgD = tc / 10;
    const int g = lane >> 3, c = lane & 7;
    u32x4 wa[4], K[2][3], V[2][3], wd[4]; uint16_t xr[4];
    const bool act = t < 480;
#pragma unroll
    for (int j = 0; j < 4; ++j) xr[j] = x[s * 80 + rgA + 20 * j];
#pragma unroll
    for (int j = 0; j < 4; ++j) wa[j] = ldnt(wq + (size_t)(s * 80 + rgA + 20 * j) * HD + h * DH + cA * 8);
    SB();
    __syncthreads();
    const size_t base = ((size_t)h * SK + (SK - WIN) + s * 128 + w * 16 + g) * DH + c * 8;
#pragma unroll
    for (int j = 0; j < 2; ++j)
#pragma unroll
      for (int u = 0; u < 3; ++u) K[j][u] = ldnt(kc + base + j * 8 * DH + u * 64);
    SB();
    {
      float acc[8];
#pragma unroll
      for (int e = 0; e < 8; ++e) acc[e] = 0.f;
#pragma unroll
      for (int j = 0; j < 4; ++j) { const float xv = bflo(xr[j]);
#pragma unroll
        for (int e = 0; e < 4; ++e) { acc[2 * e] = fmaf(xv, bflo(wa[j][e]), acc[2 * e]); acc[2 * e + 1] = fmaf(xv, bfhi(wa[j][e]), acc[2 * e + 1]); } }
      float* dst = red + (act ? rgA * 192 + cA * 8 : 48 * 80 + (t - 480) * 8);
      *reinterpret_cast<f32x4*>(dst) = f32x4{acc[0], acc[1], acc[2], acc[3]};
      *reinterpret_cast<f32x4*>(dst + 4) = f32x4{acc[4], acc[5], acc[6], acc[7]};
    }
    if (w == 0) STAMP(1);
    __syncthreads();
    __syncthreads();
#pragma unroll
    for (int i = 0; i < NV1; ++i) V[i / 3][i % 3] = ldnt(vc + base + (i / 3) * 8 * DH + (i % 3) * 64);
    SB();
    __syncthreads();
    __syncthreads();
#pragma unroll
    for (int i = NV1; i < 6; ++i) V[i / 3][i % 3] = ldnt(vc + base + (i / 3) * 8 * DH + (i % 3) * 64);
    SB();
    if (w == 0) STAMP(16);
    if (w == 7) STAMP(17);
    if (w == 3) STAMP(18);
    __syncthreads();
    if (w == 0) STAMP(2);
    u32x4 qf[3];
#pragma unroll
    for (int u = 0; u < 3; ++u) qf[u] = *reinterpret_cast<const u32x4*>(qs + u * 64 + c * 8);
    float sc[2];
#pragma unroll
    for (int j = 0; j < 2; ++j) {
      float d0 = 0.f, d1 = 0.f, d2 = 0.f;
#pragma unroll
      for (int e = 0; e < 4; ++e) { d0 = dot2(K[j][0][e], qf[0][e], d0); d1 = dot2(K[j][1][e], qf[1][e], d1); d2 = dot2(K[j][2][e], qf[2][e], d2); }
      float d = (d0 + d1) + d2;
      d += dpp<XOR1>(d); d += dpp<XOR2>(d); d += dpp<XOR7>(d);
      sc[j] = bfround(bfround(d) * 0.07216878364870322f);
    }
    float m = fmaxf(sc[0], sc[1]);
    m = fmaxf(m, dpp<XOR8>(m));
    m = x16_max(m);
    if (lane == 0) mw[w] = m;
    __syncthreads();
    {
      const f32x4 m0 = *reinterpret_cast<const f32x4*>(mw), m1 = *reinterpret_cast<const f32x4*>(mw + 4);
      m = fmaxf(fmaxf(fmaxf(m0[0], m0[1]), fmaxf(m0[2], m0[3])), fmaxf(fmaxf(m1[0], m1[1]), fmaxf(m1[2], m1[3])));
    }
    const float p0 = __builtin_amdgcn_exp2f((sc[0] - m) * LOG2E), p1 = __builtin_amdgcn_exp2f((sc[1] - m) * LOG2E);
    float l = p0 + p1;
    l += dpp<XOR8>(l);
    l = x16_sum(l);
    if (lane == 0) lw[w] = l;
    SB();
#pragma unroll
    for (int j = 0; j < NWA; ++j) wd[j] = ldnt(wo + (size_t)(h * DH + rgD + 48 * j) * DM + s * 80 + cD * 8);
    SB();
    float acc[24];
#pragma unroll
    for (int u = 0; u < 3; ++u)
#pragma unroll
      for (int e = 0; e < 4; ++e) {
        acc[u * 8 + 2 * e] = fmaf(p1, bflo(V[1][u][e]), p0 * bflo(V[0][u][e]));
        acc[u * 8 + 2 * e + 1] = fmaf(p1, bfhi(V[1][u][e]), p0 * bfhi(V[0][u][e]));
      }
    float a12[12], a6[6], a3[3];
#pragma unroll
    for (int a = 0; a < 12; ++a) { auto r = __builtin_amdgcn_permlane32_swap(__float_as_uint(acc[a]), __float_as_uint(acc[a + 12]), false, false); a12[a] = __uint_as_float(r[0]) + __uint_as_float(r[1]); }
#pragma unroll
    for (int a = 0; a < 6; ++a) { auto r = __builtin_amdgcn_permlane16_swap(__float_as_uint(a12[a]), __float_as_uint(a12[a + 6]), false, false); a6[a] = __uint_as_float(r[0]) + __uint_as_float(r[1]); }
#pragma unroll
    for (int a = 0; a < 3; ++a) { const float t1 = a6[a] + dpp<XOR8>(a6[a]), t2 = a6[a + 3] + dpp<XOR8>(a6[a + 3]); a3[a] = (lane & 8) ? t2 : t1; }
    const int ib = ((lane >> 5) & 1) * 12 + ((lane >> 4) & 1) * 6 + ((lane >> 3) & 1) * 3;
#pragma unroll
    for (int a = 0; a < 3; ++a) { const int idx = ib + a; op[w * 192 + (idx >> 3) * 64 + c * 8 + (idx & 7)] = a3[a]; }
    if (w == 0) STAMP(3);
    if (w == 7) STAMP(21);
    if (w == 4) STAMP(22);
    __syncthreads();
    __syncthreads();
#pragma unroll
    for (int j = NWA; j < 4; ++j) wd[j] = ldnt(wo + (size_t)(h * DH + rgD + 48 * j) * DM + s * 80 + cD * 8);
    SB();
    __syncthreads();
    __syncthreads();
    if (w == 0) STAMP(4);
    {
      float ac[8];
#pragma unroll
      for (int e = 0; e < 8; ++e) ac[e] = 0.f;
#pragma unroll
      for (int j = 0; j < 4; ++j) { const float ov = os[rgD + 48 * j];
#pragma unroll
        for (int e = 0; e < 4; ++e) { ac[2 * e] = fmaf(ov, bflo(wd[j][e]), ac[2 * e]); ac[2 * e + 1] = fmaf(ov, bfhi(wd[j][e]), ac[2 * e + 1]); } }
      float* dst = red + (act ? rgD * 80 + cD * 8 : 48 * 80 + (t - 480) * 8);
      *reinterpret_cast<f32x4*>(dst) = f32x4{ac[0], ac[1], ac[2], ac[3]};
      *reinterpret_cast<f32x4*>(dst + 4) = f32x4{ac[4], ac[5], ac[6], ac[7]};
    }
    if (w == 0) STAMP(5);
    __syncthreads();
  } else {
    __builtin_amdgcn_s_setprio(3);
    const int ct = t - 512;
    unsigned pfa = 0, pfb = 0; u32x4 pfv[9];
#pragma unroll
    for (int i = 0; i < 9; ++i) pfv[i] = u32x4{0u, 0u, 0u, 0u};
    if (w == 11 && b < CODEPF) {
      const char* cb = (const char*)(pc0 & ~63ull);
#pragma unroll
      for (int i = 0; i < 9; ++i) pfv[i] = *reinterpret_cast<const u32x4*>(cb + i * 1024 + lane * 16);
    }
    if (ct == 0) tagw = (__hip_atomic_load(ws + WS_EPOCH, RLX_AGENT) + 1ull);
    if (w == 8) STAMP(6);
    __syncthreads();
    __syncthreads();
    asm volatile("" :: "v"(pfa), "v"(pfb));
#pragma unroll
    for (int i = 0; i < 9; ++i) asm volatile("" :: "v"(pfv[i]));
    const u64 ep1 = tagw, tag = ep1 & 0xffffffffffull;
    u64 gq[8];
    if (ct < 192) {
      float rv[20];
#pragma unroll
      for (int r = 0; r < 20; ++r) rv[r] = red[r * 192 + ct];
      float a0 = 0.f, a1 = 0.f, a2 = 0.f, a3 = 0.f;
#pragma unroll
      for (int r = 0; r < 20; r += 4) { a0 += rv[r]; a1 += rv[r + 1]; a2 += rv[r + 2]; a3 += rv[r + 3]; }
      put_g(ws + WS_H1 + (h * 8 + s) * 192 + ct, tag, (a0 + a1) + (a2 + a3));
      if (w == 8) STAMP(7);
    }
    __syncthreads();
    __syncthreads();
    if (ct < 192) {
#pragma unroll
      for (int r = 0; r < 8; ++r) gq[r] = __hip_atomic_load(ws + WS_H1 + (h * 8 + r) * 192 + ct, RLX_AGENT);
    }
    __syncthreads();
    if (ct < 192) {
      for (unsigned spins = 0; spins < SPIN_CAP; ++spins) {
        bool ok = true;
#pragma unroll
        for (int r = 0; r < 8; ++r) ok &= chk_g(gq[r], tag);
        if (__all(ok)) break;
        if (w == 8) STAMP(13);
#pragma unroll
        for (int r = 0; r < 8; ++r) gq[r] = __hip_atomic_load(ws + WS_H1 + (h * 8 + r) * 192 + ct, RLX_AGENT);
      }
      if (w == 8) STAMP(12);
      if (w == 9) STAMP(14);
      if (w == 10) STAMP(15);
      float q = 0.f;
#pragma unroll
      for (int r = 0; r < 8; ++r) q += val_g(gq[r]);
      qs[ct] = (uint16_t)(cvtpk(q, q) & 0xffffu);
    }
    __syncthreads();
    __syncthreads();
    if (w == 8) STAMP(19);
    __syncthreads();
    if (w == 8) STAMP(20);
    {
      gu64* dst = ws + WS_H2 + (h * 8 + s) * 200;
      if (ct < 192) {
        float ov[8];
#pragma unroll
        for (int r = 0; r < 8; ++r) ov[r] = op[r * 192 + ct];
        put_g(dst + ct, tag, ((ov[0] + ov[1]) + (ov[2] + ov[3])) + ((ov[4] + ov[5]) + (ov[6] + ov[7])));
      } else if (ct < 194) {
        const f32x4 m0 = *reinterpret_cast<const f32x4*>(mw), m1 = *reinterpret_cast<const f32x4*>(mw + 4);
        const f32x4 l0 = *reinterpret_cast<const f32x4*>(lw), l1 = *reinterpret_cast<const f32x4*>(lw + 4);
        const float M = fmaxf(fmaxf(fmaxf(m0[0], m0[1]), fmaxf(m0[2], m0[3])), fmaxf(fmaxf(m1[0], m1[1]), fmaxf(m1[2], m1[3])));
        const float L = ((l0[0] + l0[1]) + (l0[2] + l0[3])) + ((l1[0] + l1[1]) + (l1[2] + l1[3]));
        put_g(dst + ct, tag, ct == 192 ? M : L);
      }
    }
    if (w == 8) STAMP(10);
    __syncthreads();
    __syncthreads();
    if (ct < 192) {
      u64 go[8], gs;
      const int sr = (lane >> 1) & 7, sw = lane & 1;
      for (unsigned spins = 0; spins < SPIN_CAP; ++spins) {
        bool ok = true;
        gs = __hip_atomic_load(ws + WS_H2 + (h * 8 + sr) * 200 + 192 + sw, RLX_AGENT); ok &= chk_g(gs, tag);
#pragma unroll
        for (int r = 0; r < 8; ++r) { go[r] = __hip_atomic_load(ws + WS_H2 + (h * 8 + r) * 200 + ct, RLX_AGENT); ok &= chk_g(go[r], tag); }
        if (__all(ok)) break;
        __builtin_amdgcn_s_sleep(1);
      }
      if (w == 8) STAMP(11);
      const float sv = val_g(gs);
      float mr[8], lr[8];
#pragma unroll
      for (int r = 0; r < 8; ++r) { mr[r] = __builtin_bit_cast(float, __builtin_amdgcn_readlane(__builtin_bit_cast(int, sv), 2 * r)); lr[r] = __builtin_bit_cast(float, __builtin_amdgcn_readlane(__builtin_bit_cast(int, sv), 2 * r + 1)); }
      float M = mr[0];
#pragma unroll
      for (int r = 1; r < 8; ++r) M = fmaxf(M, mr[r]);
      float L = 0.f, o = 0.f;
#pragma unroll
      for (int r = 0; r < 8; ++r) { const float f = __builtin_amdgcn_exp2f((mr[r] - M) * LOG2E); L = fmaf(f, lr[r], L); o = fmaf(f, val_g(go[r]), o); }
      os[ct] = bfround(o / L);
    }
    __syncthreads();
    __syncthreads();
    if (ct < 80) {
      float rv[48];
#pragma unroll
      for (int r = 0; r < 48; ++r) rv[r] = red[r * 80 + ct];
      float a0 = 0.f, a1 = 0.f, a2 = 0.f, a3 = 0.f;
#pragma unroll
      for (int r = 0; r < 48; r += 4) { a0 += rv[r]; a1 += rv[r + 1]; a2 += rv[r + 2]; a3 += rv[r + 3]; }
      put_g(ws + WS_H3 + (s * 32 + h) * 80 + ct, tag, (a0 + a1) + (a2 + a3));
    }
    if (w == 8) STAMP(8);
    if (h < 10 && w == 11) {
      const int r0 = lane >> 3, col = lane & 7;
      u64 gf[4];
      for (unsigned spins = 0; spins < SPIN_CAP; ++spins) {
        bool ok = true;
#pragma unroll
        for (int k = 0; k < 4; ++k) { gf[k] = __hip_atomic_load(ws + WS_H3 + (s * 32 + r0 + 8 * k) * 80 + h * 8 + col, RLX_AGENT); ok &= chk_g(gf[k], tag); }
        if (__all(ok)) break;
        __builtin_amdgcn_s_sleep(1);
      }
      float a = (val_g(gf[0]) + val_g(gf[1])) + (val_g(gf[2]) + val_g(gf[3]));
      a += dpp<XOR8>(a);
      a = x16_sum(a);
      if (lane < 8) out[s * 80 + h * 8 + lane] = (uint16_t)(cvtpk(a, a) & 0xffffu);
      STAMP(9);
      if (b == 0 && lane == 0) __hip_atomic_store(ws + WS_EPOCH, ep1, RLX_AGENT);
      { const volatile u64* vs = stamps; const u64 d = vs[PROBE] - vs[0]; const u64 tgt = __builtin_amdgcn_s_memrealtime() + 4 * (d > 5000 ? 0 : d);
        for (int i = 0; i < 1000000; ++i) { if (__builtin_amdgcn_s_memrealtime() >= tgt) break; __builtin_amdgcn_s_sleep(1); } }
    }
  }
  STAMP_DUMP;
}

extern "C" void kernel_launch(void* const* d_in, const int* in_sizes, int n_in, void* d_out, int out_size, void* d_ws, size_t ws_size, hipStream_t stream) {
  fused<<<256, 768, 0, stream>>>((const uint16_t*)d_in[0], (const uint16_t*)d_in[1], (const uint16_t*)d_in[2], (const uint16_t*)d_in[3], (const uint16_t*)d_in[4],
                                 (uint16_t*)d_out, (u64*)d_ws);
}
```

```cpp
#include <hip/hip_runtime.h>
#include <hip/hip_bf16.h>
#include <stdint.h>

typedef unsigned u32x4 __attribute__((ext_vector_type(4)));
typedef float f32x4 __attribute__((ext_vector_type(4)));
typedef __bf16 bf16x2_t __attribute__((ext_vector_type(2)));
typedef unsigned long long u64;
typedef __attribute__((address_space(1))) u64 gu64;

constexpr int DM = 640, NH = 32, SK = 4096, DH = 192, WIN = 1024, NS = 8;
constexpr int HD = NH * DH;
#define CODEPF 64
#define TLBPF 0
#define NWA 4
#ifndef NV1
#define NV1 2
#endif
constexpr unsigned SPIN_CAP = 1u << 18;

constexpr int WS_EPOCH = 0;
constexpr int WS_H1 = 64;
constexpr int WS_H2 = WS_H1 + NH * NS * 192;
constexpr int WS_H3 = WS_H2 + NH * NS * 200;

__device__ __forceinline__ float bflo(unsigned u) { return __uint_as_float(u << 16); }
__device__ __forceinline__ float bfhi(unsigned u) { return __uint_as_float(u & 0xffff0000u); }
__device__ __forceinline__ float dot2(unsigned k, unsigned q, float acc) {
  return __builtin_amdgcn_fdot2_f32_bf16(__builtin_bit_cast(bf16x2_t, k), __builtin_bit_cast(bf16x2_t, q), acc, false);
}
__device__ __forceinline__ unsigned cvtpk(float lo, float hi) {
  unsigned r; asm volatile("v_cvt_pk_bf16_f32 %0, %1, %2" : "=v"(r) : "v"(lo), "v"(hi)); return r;
}
__device__ __forceinline__ float bfround(float x) { return bflo(cvtpk(x, x)); }
template <int CTRL> __device__ __forceinline__ float dpp(float x) {
  return __builtin_bit_cast(float, __builtin_amdgcn_mov_dpp(__builtin_bit_cast(int, x), CTRL, 0xf, 0xf, true));
}
constexpr int XOR1 = 0xB1, XOR2 = 0x4E, XOR7 = 0x141, XOR8 = 0x128;
__device__ __forceinline__ u32x4 ldnt(const void* p) { return __builtin_nontemporal_load(reinterpret_cast<const u32x4*>(p)); }
__device__ __forceinline__ float x16_sum(float l) {
  { auto r = __builtin_amdgcn_permlane16_swap(__float_as_uint(l), __float_as_uint(l), false, false); l = __uint_as_float(r[0]) + __uint_as_float(r[1]); }
  { auto r = __builtin_amdgcn_permlane32_swap(__float_as_uint(l), __float_as_uint(l), false, false); l = __uint_as_float(r[0]) + __uint_as_float(r[1]); }
  return l;
}
__device__ __forceinline__ float x16_max(float m) {
  { auto r = __builtin_amdgcn_permlane16_swap(__float_as_uint(m), __float_as_uint(m), false, false); m = fmaxf(__uint_as_float(r[0]), __uint_as_float(r[1])); }
  { auto r = __builtin_amdgcn_permlane32_swap(__float_as_uint(m), __float_as_uint(m), false, false); m = fmaxf(__uint_as_float(r[0]), __uint_as_float(r[1])); }
  return m;
}
#define RLX_AGENT __ATOMIC_RELAXED, __HIP_MEMORY_SCOPE_AGENT
__device__ __forceinline__ void put_g(gu64* g, u64 tag, float v) {
  const unsigned hi = ((__float_as_uint(v) + 0x80u) & 0xffffff00u) | (unsigned)(tag >> 32);
  __hip_atomic_store(g, ((u64)hi << 32) | (unsigned)tag, RLX_AGENT);
}
__device__ __forceinline__ bool chk_g(u64 x, u64 tag) { return (x & 0xffffffffffull) == tag; }
__device__ __forceinline__ float val_g(u64 x) { return __uint_as_float((unsigned)(x >> 32) & 0xffffff00u); }

#define STAMP(i)
#define STAMP_DECL
#define STAMP_DUMP
constexpr float LOG2E = 1.4426950408889634f;
#define SB() __builtin_amdgcn_sched_barrier(0)

__global__ void __launch_bounds__(768) fused(const uint16_t* __restrict__ x, const uint16_t* __restrict__ kc, const uint16_t* __restrict__ vc,
                                             const uint16_t* __restrict__ wq, const uint16_t* __restrict__ wo, uint16_t* __restrict__ out, u64* ws_) {
  __shared__ __attribute__((aligned(16))) float red[48 * 80 + 32 * 8];
  __shared__ __attribute__((aligned(16))) float op[8 * 192];
  __shared__ __attribute__((aligned(16))) uint16_t qs[192];
  __shared__ __attribute__((aligned(16))) float os[192];
  __shared__ __attribute__((aligned(16))) float mw[8];
  __shared__ __attribute__((aligned(16))) float lw[8];
  __shared__ u64 tagw;
  STAMP_DECL
  u64 pc0; asm volatile("s_getpc_b64 %0" : "=s"(pc0));
  gu64* ws = (gu64*)ws_;
  const int b = blockIdx.x, jj = b >> 3;
  const int h = (b & 7) * 4 + (jj >> 3), s = jj & 7;
  const int t = threadIdx.x, w = t >> 6, lane = t & 63;
  if (w < 8) {
    if (w == 0) STAMP(0);
    const int tc = t < 480 ? t : t - 32;
    const int cA = tc % 24, rgA = tc / 24, cD = tc % 10, rgD = tc / 10;
    const int g = lane >> 3, c = lane & 7;
    u32x4 wa[4], K[2][3], V[2][3], wd[4]; uint16_t xr[4];
    const bool act = t < 480;
#pragma unroll
    for (int j = 0; j < 4; ++j) xr[j] = x[s * 80 + rgA + 20 * j];
#pragma unroll
    for (int j = 0; j < 4; ++j) wa[j] = ldnt(wq + (size_t)(s * 80 + rgA + 20 * j) * HD + h * DH + cA * 8);
    SB();
    __syncthreads();
    const size_t base = ((size_t)h * SK + (SK - WIN) + s * 128 + w * 16 + g) * DH + c * 8;
#pragma unroll
    for (int j = 0; j < 2; ++j)
#pragma unroll
      for (int u = 0; u < 3; ++u) K[j][u] = ldnt(kc + base + j * 8 * DH + u * 64);
    SB();
    {
      float acc[8];
#pragma unroll
      for (int e = 0; e < 8; ++e) acc[e] = 0.f;
#pragma unroll
      for (int j = 0; j < 4; ++j) { const float xv = bflo(xr[j]);
#pragma unroll
        for (int e = 0; e < 4; ++e) { acc[2 * e] = fmaf(xv, bflo(wa[j][e]), acc[2 * e]); acc[2 * e + 1] = fmaf(xv, bfhi(wa[j][e]), acc[2 * e + 1]); } }
      float* dst = red + (act ? rgA * 192 + cA * 8 : 48 * 80 + (t - 480) * 8);
      *reinterpret_cast<f32x4*>(dst) = f32x4{acc[0], acc[1], acc[2], acc[3]};
      *reinterpret_cast<f32x4*>(dst + 4) = f32x4{acc[4], acc[5], acc[6], acc[7]};
    }
    if (w == 0) STAMP(1);
    __syncthreads();
    __syncthreads();
#pragma unroll
    for (int i = 0; i < NV1; ++i) V[i / 3][i % 3] = ldnt(vc + base + (i / 3) * 8 * DH + (i % 3) * 64);
    SB();
    __syncthreads();
    __syncthreads();
#pragma unroll
    for (int i = NV1; i < 6; ++i) V[i / 3][i % 3] = ldnt(vc + base + (i / 3) * 8 * DH + (i % 3) * 64);
    SB();
    if (w == 0) STAMP(16);
    if (w == 7) STAMP(17);
    if (w == 3) STAMP(18);
    __syncthreads();
    if (w == 0) STAMP(2);
    u32x4 qf[3];
#pragma unroll
    for (int u = 0; u < 3; ++u) qf[u] = *reinterpret_cast<const u32x4*>(qs + u * 64 + c * 8);
    float sc[2];
#pragma unroll
    for (int j = 0; j < 2; ++j) {
      float d0 = 0.f, d1 = 0.f, d2 = 0.f;
#pragma unroll
      for (int e = 0; e < 4; ++e) { d0 = dot2(K[j][0][e], qf[0][e], d0); d1 = dot2(K[j][1][e], qf[1][e], d1); d2 = dot2(K[j][2][e], qf[2][e], d2); }
      float d = (d0 + d1) + d2;
      d += dpp<XOR1>(d); d += dpp<XOR2>(d); d += dpp<XOR7>(d);
      sc[j] = bfround(bfround(d) * 0.07216878364870322f);
    }
    float m = fmaxf(sc[0], sc[1]);
    m = fmaxf(m, dpp<XOR8>(m));
    m = x16_max(m);
    if (lane == 0) mw[w] = m;
    __syncthreads();
    {
      const f32x4 m0 = *reinterpret_cast<const f32x4*>(mw), m1 = *reinterpret_cast<const f32x4*>(mw + 4);
      m = fmaxf(fmaxf(fmaxf(m0[0], m0[1]), fmaxf(m0[2], m0[3])), fmaxf(fmaxf(m1[0], m1[1]), fmaxf(m1[2], m1[3])));
    }
    const float p0 = __builtin_amdgcn_exp2f((sc[0] - m) * LOG2E), p1 = __builtin_amdgcn_exp2f((sc[1] - m) * LOG2E);
    float l = p0 + p1;
    l += dpp<XOR8>(l);
    l = x16_sum(l);
    if (lane == 0) lw[w] = l;
    SB();
#pragma unroll
    for (int j = 0; j < NWA; ++j) wd[j] = ldnt(wo + (size_t)(h * DH + rgD + 48 * j) * DM + s * 80 + cD * 8);
    SB();
    float acc[24];
#pragma unroll
    for (int u = 0; u < 3; ++u)
#pragma unroll
      for (int e = 0; e < 4; ++e) {
        acc[u * 8 + 2 * e] = fmaf(p1, bflo(V[1][u][e]), p0 * bflo(V[0][u][e]));
        acc[u * 8 + 2 * e + 1] = fmaf(p1, bfhi(V[1][u][e]), p0 * bfhi(V[0][u][e]));
      }
    float a12[12], a6[6], a3[3];
#pragma unroll
    for (int a = 0; a < 12; ++a) { auto r = __builtin_amdgcn_permlane32_swap(__float_as_uint(acc[a]), __float_as_uint(acc[a + 12]), false, false); a12[a] = __uint_as_float(r[0]) + __uint_as_float(r[1]); }
#pragma unroll
    for (int a = 0; a < 6; ++a) { auto r = __builtin_amdgcn_permlane16_swap(__float_as_uint(a12[a]), __float_as_uint(a12[a + 6]), false, false); a6[a] = __uint_as_float(r[0]) + __uint_as_float(r[1]); }
#pragma unroll
    for (int a = 0; a < 3; ++a) { const float t1 = a6[a] + dpp<XOR8>(a6[a]), t2 = a6[a + 3] + dpp<XOR8>(a6[a + 3]); a3[a] = (lane & 8) ? t2 : t1; }
    const int ib = ((lane >> 5) & 1) * 12 + ((lane >> 4) & 1) * 6 + ((lane >> 3) & 1) * 3;
#pragma unroll
    for (int a = 0; a < 3; ++a) { const int idx = ib + a; op[w * 192 + (idx >> 3) * 64 + c * 8 + (idx & 7)] = a3[a]; }
    if (w == 0) STAMP(3);
    if (w == 7) STAMP(21);
    if (w == 4) STAMP(22);
    __syncthreads();
    __syncthreads();
#pragma unroll
    for (int j = NWA; j < 4; ++j) wd[j] = ldnt(wo + (size_t)(h * DH + rgD + 48 * j) * DM + s * 80 + cD * 8);
    SB();
    __syncthreads();
    __syncthreads();
    if (w == 0) STAMP(4);
    {
      float ac[8];
#pragma unroll
      for (int e = 0; e < 8; ++e) ac[e] = 0.f;
#pragma unroll
      for (int j = 0; j < 4; ++j) { const float ov = os[rgD + 48 * j];
#pragma unroll
        for (int e = 0; e < 4; ++e) { ac[2 * e] = fmaf(ov, bflo(wd[j][e]), ac[2 * e]); ac[2 * e + 1] = fmaf(ov, bfhi(wd[j][e]), ac[2 * e + 1]); } }
      float* dst = red + (act ? rgD * 80 + cD * 8 : 48 * 80 + (t - 480) * 8);
      *reinterpret_cast<f32x4*>(dst) = f32x4{ac[0], ac[1], ac[2], ac[3]};
      *reinterpret_cast<f32x4*>(dst + 4) = f32x4{ac[4], ac[5], ac[6], ac[7]};
    }
    if (w == 0) STAMP(5);
    __syncthreads();
  } else {
    __builtin_amdgcn_s_setprio(3);
    const int ct = t - 512;
    unsigned pfa = 0, pfb = 0; u32x4 pfv[12];
#pragma unroll
    for (int i = 0; i < 12; ++i) pfv[i] = u32x4{0u, 0u, 0u, 0u};
    if (w == 11 && b < CODEPF) {
      const char* cb = (const char*)(pc0 & ~63ull);
#pragma unroll
      for (int i = 0; i < 12; ++i) pfv[i] = *reinterpret_cast<const __attribute__((address_space(1))) u32x4*>((const __attribute__((address_space(1))) char*)cb + i * 1024 + lane * 16);
    }
    if (ct == 0) tagw = (__hip_atomic_load(ws + WS_EPOCH, RLX_AGENT) + 1ull);
    if (w == 8) STAMP(6);
    __syncthreads();
    __syncthreads();
    asm volatile("" :: "v"(pfa), "v"(pfb));
#pragma unroll
    for (int i = 0; i < 12; ++i) asm volatile("" :: "v"(pfv[i]));
    const u64 ep1 = tagw, tag = ep1 & 0xffffffffffull;
    u64 gq[8];
    if (ct < 192) {
      float rv[20];
#pragma unroll
      for (int r = 0; r < 20; ++r) rv[r] = red[r * 192 + ct];
      float a0 = 0.f, a1 = 0.f, a2 = 0.f, a3 = 0.f;
#pragma unroll
      for (int r = 0; r < 20; r += 4) { a0 += rv[r]; a1 += rv[r + 1]; a2 += rv[r + 2]; a3 += rv[r + 3]; }
      put_g(ws + WS_H1 + (h * 8 + s) * 192 + ct, tag, (a0 + a1) + (a2 + a3));
      if (w == 8) STAMP(7);
    }
    __syncthreads();
    __syncthreads();
    if (ct < 192) {
#pragma unroll
      for (int r = 0; r < 8; ++r) gq[r] = __hip_atomic_load(ws + WS_H1 + (h * 8 + r) * 192 + ct, RLX_AGENT);
    }
    __syncthreads();
    if (ct < 192) {
      for (unsigned spins = 0; spins < SPIN_CAP; ++spins) {
        bool ok = true;
#pragma unroll
        for (int r = 0; r < 8; ++r) ok &= chk_g(gq[r], tag);
        if (__all(ok)) break;
        if (w == 8) STAMP(13);
#pragma unroll
        for (int r = 0; r < 8; ++r) gq[r] = __hip_atomic_load(ws + WS_H1 + (h * 8 + r) * 192 + ct, RLX_AGENT);
      }
      if (w == 8) STAMP(12);
      if (w == 9) STAMP(14);
      if (w == 10) STAMP(15);
      float q = 0.f;
#pragma unroll
      for (int r = 0; r < 8; ++r) q += val_g(gq[r]);
      qs[ct] = (uint16_t)(cvtpk(q, q) & 0xffffu);
    }
    __syncthreads();
    __syncthreads();
    if (w == 8) STAMP(19);
    __syncthreads();
    if (w == 8) STAMP(20);
    {
      gu64* dst = ws + WS_H2 + (h * 8 + s) * 200;
      if (ct < 192) {
        float ov[8];
#pragma unroll
        for (int r = 0; r < 8; ++r) ov[r] = op[r * 192 + ct];
        put_g(dst + ct, tag, ((ov[0] + ov[1]) + (ov[2] + ov[3])) + ((ov[4] + ov[5]) + (ov[6] + ov[7])));
      } else if (ct < 194) {
        const f32x4 m0 = *reinterpret_cast<const f32x4*>(mw), m1 = *reinterpret_cast<const f32x4*>(mw + 4);
        const f32x4 l0 = *reinterpret_cast<const f32x4*>(lw), l1 = *reinterpret_cast<const f32x4*>(lw + 4);
        const float M = fmaxf(fmaxf(fmaxf(m0[0], m0[1]), fmaxf(m0[2], m0[3])), fmaxf(fmaxf(m1[0], m1[1]), fmaxf(m1[2], m1[3])));
        const float L = ((l0[0] + l0[1]) + (l0[2] + l0[3])) + ((l1[0] + l1[1]) + (l1[2] + l1[3]));
        put_g(dst + ct, tag, ct == 192 ? M : L);
      }
    }
    if (w == 8) STAMP(10);
    __syncthreads();
    __syncthreads();
    if (ct < 192) {
      u64 go[8], gs;
      const int sr = (lane >> 1) & 7, sw = lane & 1;
      for (unsigned spins = 0; spins < SPIN_CAP; ++spins) {
        bool ok = true;
        gs = __hip_atomic_load(ws + WS_H2 + (h * 8 + sr) * 200 + 192 + sw, RLX_AGENT); ok &= chk_g(gs, tag);
#pragma unroll
        for (int r = 0; r < 8; ++r) { go[r] = __hip_atomic_load(ws + WS_H2 + (h * 8 + r) * 200 + ct, RLX_AGENT); ok &= chk_g(go[r], tag); }
        if (__all(ok)) break;
        __builtin_amdgcn_s_sleep(1);
      }
      if (w == 8) STAMP(11);
      const float sv = val_g(gs);
      float mr[8], lr[8];
#pragma unroll
      for (int r = 0; r < 8; ++r) { mr[r] = __builtin_bit_cast(float, __builtin_amdgcn_readlane(__builtin_bit_cast(int, sv), 2 * r)); lr[r] = __builtin_bit_cast(float, __builtin_amdgcn_readlane(__builtin_bit_cast(int, sv), 2 * r + 1)); }
      float M = mr[0];
#pragma unroll
      for (int r = 1; r < 8; ++r) M = fmaxf(M, mr[r]);
      float L = 0.f, o = 0.f;
#pragma unroll
      for (int r = 0; r < 8; ++r) { const float f = __builtin_amdgcn_exp2f((mr[r] - M) * LOG2E); L = fmaf(f, lr[r], L); o = fmaf(f, val_g(go[r]), o); }
      os[ct] = bfround(o / L);
    }
    __syncthreads();
    __syncthreads();
    if (ct < 80) {
      float rv[48];
#pragma unroll
      for (int r = 0; r < 48; ++r) rv[r] = red[r * 80 + ct];
      float a0 = 0.f, a1 = 0.f, a2 = 0.f, a3 = 0.f;
#pragma unroll
      for (int r = 0; r < 48; r += 4) { a0 += rv[r]; a1 += rv[r + 1]; a2 += rv[r + 2]; a3 += rv[r + 3]; }
      put_g(ws + WS_H3 + (s * 32 + h) * 80 + ct, tag, (a0 + a1) + (a2 + a3));
    }
    if (w == 8) STAMP(8);
    if (h < 10 && w == 11) {
      const int r0 = lane >> 3, col = lane & 7;
      u64 gf[4];
      for (unsigned spins = 0; spins < SPIN_CAP; ++spins) {
        bool ok = true;
#pragma unroll
        for (int k = 0; k < 4; ++k) { gf[k] = __hip_atomic_load(ws + WS_H3 + (s * 32 + r0 + 8 * k) * 80 + h * 8 + col, RLX_AGENT); ok &= chk_g(gf[k], tag); }
        if (__all(ok)) break;
        __builtin_amdgcn_s_sleep(1);
      }
      float a = (val_g(gf[0]) + val_g(gf[1])) + (val_g(gf[2]) + val_g(gf[3]));
      a += dpp<XOR8>(a);
      a = x16_sum(a);
      if (lane < 8) out[s * 80 + h * 8 + lane] = (uint16_t)(cvtpk(a, a) & 0xffffu);
      STAMP(9);
      if (b == 0 && lane == 0) __hip_atomic_store(ws + WS_EPOCH, ep1, RLX_AGENT);
    }
  }
  STAMP_DUMP;
}

extern "C" void kernel_launch(void* const* d_in, const int* in_sizes, int n_in, void* d_out, int out_size, void* d_ws, size_t ws_size, hipStream_t stream) {
  fused<<<256, 768, 0, stream>>>((const uint16_t*)d_in[0], (const uint16_t*)d_in[1], (const uint16_t*)d_in[2], (const uint16_t*)d_in[3], (const uint16_t*)d_in[4],
                                 (uint16_t*)d_out, (u64*)d_ws);
}
```

```cpp
#include <hip/hip_runtime.h>
#include <hip/hip_bf16.h>
#include <stdint.h>

typedef unsigned u32x4 __attribute__((ext_vector_type(4)));
typedef float f32x4 __attribute__((ext_vector_type(4)));
typedef __bf16 bf16x2_t __attribute__((ext_vector_type(2)));
typedef unsigned long long u64;
typedef __attribute__((address_space(1))) u64 gu64;

constexpr int DM = 640, NH = 32, SK = 4096, DH = 192, WIN = 1024, NS = 8;
constexpr int HD = NH * DH;
#ifndef NV1
#define NV1 2
#endif
#define NDRY 0
constexpr unsigned SPIN_CAP = 1u << 18;

constexpr int WS_EPOCH = 0;
constexpr int WS_H1 = 64;
constexpr int WS_H2 = WS_H1 + NH * NS * 192;
constexpr int WS_H3 = WS_H2 + NH * NS * 200;

__device__ __forceinline__ float bflo(unsigned u) { return __uint_as_float(u << 16); }
__device__ __forceinline__ float bfhi(unsigned u) { return __uint_as_float(u & 0xffff0000u); }
__device__ __forceinline__ float dot2(unsigned k, unsigned q, float acc) {
  return __builtin_amdgcn_fdot2_f32_bf16(__builtin_bit_cast(bf16x2_t, k), __builtin_bit_cast(bf16x2_t, q), acc, false);
}
__device__ __forceinline__ unsigned cvtpk(float lo, float hi) {
  unsigned r; asm volatile("v_cvt_pk_bf16_f32 %0, %1, %2" : "=v"(r) : "v"(lo), "v"(hi)); return r;
}
__device__ __forceinline__ float bfround(float x) { return bflo(cvtpk(x, x)); }
template <int CTRL> __device__ __forceinline__ float dpp(float x) {
  return __builtin_bit_cast(float, __builtin_amdgcn_mov_dpp(__builtin_bit_cast(int, x), CTRL, 0xf, 0xf, true));
}
constexpr int XOR1 = 0xB1, XOR2 = 0x4E, XOR7 = 0x141, XOR8 = 0x128;
#define LDNT(r, p) asm volatile("global_load_dwordx4 %0, %1, off nt" : "=v"(r) : "v"(p) : "memory")
#define LDU16(r, p) asm volatile("global_load_ushort %0, %1, off" : "=v"(r) : "v"(p) : "memory")
__device__ __forceinline__ float x16_sum(float l) {
  { auto r = __builtin_amdgcn_permlane16_swap(__float_as_uint(l), __float_as_uint(l), false, false); l = __uint_as_float(r[0]) + __uint_as_float(r[1]); }
  { auto r = __builtin_amdgcn_permlane32_swap(__float_as_uint(l), __float_as_uint(l), false, false); l = __uint_as_float(r[0]) + __uint_as_float(r[1]); }
  return l;
}
__device__ __forceinline__ float x16_max(float m) {
  { auto r = __builtin_amdgcn_permlane16_swap(__float_as_uint(m), __float_as_uint(m), false, false); m = fmaxf(__uint_as_float(r[0]), __uint_as_float(r[1])); }
  { auto r = __builtin_amdgcn_permlane32_swap(__float_as_uint(m), __float_as_uint(m), false, false); m = fmaxf(__uint_as_float(r[0]), __uint_as_float(r[1])); }
  return m;
}
#define RLX_AGENT __ATOMIC_RELAXED, __HIP_MEMORY_SCOPE_AGENT
__device__ __forceinline__ void put_g(gu64* g, u64 tag, float v) {
  const unsigned hi = ((__float_as_uint(v) + 0x80u) & 0xffffff00u) | (unsigned)(tag >> 32);
  __hip_atomic_store(g, ((u64)hi << 32) | (unsigned)tag, RLX_AGENT);
}
__device__ __forceinline__ bool chk_g(u64 x, u64 tag) { return (x & 0xffffffffffull) == tag; }
__device__ __forceinline__ float val_g(u64 x) { return __uint_as_float((unsigned)(x >> 32) & 0xffffff00u); }

#define STAMP(i)
#define STAMP_DECL
#define STAMP_DUMP
constexpr float LOG2E = 1.4426950408889634f;
#define SB() __builtin_amdgcn_sched_barrier(0)

__global__ void __launch_bounds__(768) fused(const uint16_t* __restrict__ x, const uint16_t* __restrict__ kc, const uint16_t* __restrict__ vc,
                                             const uint16_t* __restrict__ wq, const uint16_t* __restrict__ wo, uint16_t* __restrict__ out, u64* ws_) {
  __shared__ __attribute__((aligned(16))) float red[48 * 80 + 32 * 8];
  __shared__ __attribute__((aligned(16))) float op[8 * 192];
  __shared__ __attribute__((aligned(16))) uint16_t qs[192];
  __shared__ __attribute__((aligned(16))) float os[192];
  __shared__ __attribute__((aligned(16))) float mw[8];
  __shared__ __attribute__((aligned(16))) float lw[8];
  __shared__ u64 tagw;
  STAMP_DECL
  gu64* ws = (gu64*)ws_;
  const bool real = blockIdx.x < 256;
  const int b = blockIdx.x & 255, jj = b >> 3;
  const int h = (b & 7) * 4 + (jj >> 3), s = jj & 7;
  const int t = threadIdx.x, w = __builtin_amdgcn_readfirstlane(t >> 6), lane = t & 63;
  if (w < 8) {
    if (w == 0) STAMP(0);
    const int tc = t < 480 ? t : t - 32;
    const int cA = tc % 24, rgA = tc / 24, cD = tc % 10, rgD = tc / 10;
    const int g = lane >> 3, c = lane & 7;
    u32x4 wa[4], K[6], V[6], wd[4]; unsigned xr[4];
    const bool act = t < 480;
    const size_t base = ((size_t)h * SK + (SK - WIN) + s * 128 + w * 16 + g) * DH + c * 8;
    if (real) {
#pragma unroll
      for (int j = 0; j < 4; ++j) LDNT(wa[j], wq + (size_t)(s * 80 + rgA + 20 * j) * HD + h * DH + cA * 8);
#pragma unroll
      for (int j = 0; j < 4; ++j) LDU16(xr[j], x + s * 80 + rgA + 20 * j);
    }
    __syncthreads();
    if (real) {
#pragma unroll
      for (int i = 0; i < 6; ++i) LDNT(K[i], kc + base + (i / 3) * 8 * DH + (i % 3) * 64);
    }
    asm volatile("s_waitcnt vmcnt(6)" : "+v"(wa[0]), "+v"(wa[1]), "+v"(wa[2]), "+v"(wa[3]), "+v"(xr[0]), "+v"(xr[1]), "+v"(xr[2]), "+v"(xr[3]) :: "memory");
    {
      float acc[8];
#pragma unroll
      for (int e = 0; e < 8; ++e) acc[e] = 0.f;
#pragma unroll
      for (int j = 0; j < 4; ++j) { const float xv = bflo(xr[j]);
#pragma unroll
        for (int e = 0; e < 4; ++e) { acc[2 * e] = fmaf(xv, bflo(wa[j][e]), acc[2 * e]); acc[2 * e + 1] = fmaf(xv, bfhi(wa[j][e]), acc[2 * e + 1]); } }
      float* dst = red + (act ? rgA * 192 + cA * 8 : 48 * 80 + (t - 480) * 8);
      *reinterpret_cast<f32x4*>(dst) = f32x4{acc[0], acc[1], acc[2], acc[3]};
      *reinterpret_cast<f32x4*>(dst + 4) = f32x4{acc[4], acc[5], acc[6], acc[7]};
    }
    if (w == 0) STAMP(1);
    __syncthreads();
    __syncthreads();
    if (real) {
#pragma unroll
      for (int i = 0; i < NV1; ++i) LDNT(V[i], vc + base + (i / 3) * 8 * DH + (i % 3) * 64);
    }
    __syncthreads();
    __syncthreads();
    if (real) {
#pragma unroll
      for (int i = NV1; i < 6; ++i) LDNT(V[i], vc + base + (i / 3) * 8 * DH + (i % 3) * 64);
    }
    __syncthreads();
    if (w == 0) STAMP(2);
    u32x4 qf[3];
#pragma unroll
    for (int u = 0; u < 3; ++u) qf[u] = *reinterpret_cast<const u32x4*>(qs + u * 64 + c * 8);
    asm volatile("s_waitcnt vmcnt(6)" : "+v"(K[0]), "+v"(K[1]), "+v"(K[2]), "+v"(K[3]), "+v"(K[4]), "+v"(K[5]) :: "memory");
    float sc[2];
#pragma unroll
    for (int j = 0; j < 2; ++j) {
      float d0 = 0.f, d1 = 0.f, d2 = 0.f;
#pragma unroll
      for (int e = 0; e < 4; ++e) { d0 = dot2(K[j * 3][e], qf[0][e], d0); d1 = dot2(K[j * 3 + 1][e], qf[1][e], d1); d2 = dot2(K[j * 3 + 2][e], qf[2][e], d2); }
      float d = (d0 + d1) + d2;
      d += dpp<XOR1>(d); d += dpp<XOR2>(d); d += dpp<XOR7>(d);
      sc[j] = bfround(bfround(d) * 0.07216878364870322f);
    }
    float m = fmaxf(sc[0], sc[1]);
    m = fmaxf(m, dpp<XOR8>(m));
    m = x16_max(m);
    if (lane == 0) mw[w] = m;
    __syncthreads();
    {
      const f32x4 m0 = *reinterpret_cast<const f32x4*>(mw), m1 = *reinterpret_cast<const f32x4*>(mw + 4);
      m = fmaxf(fmaxf(fmaxf(m0[0], m0[1]), fmaxf(m0[2], m0[3])), fmaxf(fmaxf(m1[0], m1[1]), fmaxf(m1[2], m1[3])));
    }
    const float p0 = __builtin_amdgcn_exp2f((sc[0] - m) * LOG2E), p1 = __builtin_amdgcn_exp2f((sc[1] - m) * LOG2E);
    float l = p0 + p1;
    l += dpp<XOR8>(l);
    l = x16_sum(l);
    if (lane == 0) lw[w] = l;
    if (real) {
#pragma unroll
      for (int j = 0; j < 4; ++j) LDNT(wd[j], wo + (size_t)(h * DH + rgD + 48 * j) * DM + s * 80 + cD * 8);
    }
    asm volatile("s_waitcnt vmcnt(4)" : "+v"(V[0]), "+v"(V[1]), "+v"(V[2]), "+v"(V[3]), "+v"(V[4]), "+v"(V[5]) :: "memory");
    float acc[24];
#pragma unroll
    for (int u = 0; u < 3; ++u)
#pragma unroll
      for (int e = 0; e < 4; ++e) {
        acc[u * 8 + 2 * e] = fmaf(p1, bflo(V[3 + u][e]), p0 * bflo(V[u][e]));
        acc[u * 8 + 2 * e + 1] = fmaf(p1, bfhi(V[3 + u][e]), p0 * bfhi(V[u][e]));
      }
    float a12[12], a6[6], a3[3];
#pragma unroll
    for (int a = 0; a < 12; ++a) { auto r = __builtin_amdgcn_permlane32_swap(__float_as_uint(acc[a]), __float_as_uint(acc[a + 12]), false, false); a12[a] = __uint_as_float(r[0]) + __uint_as_float(r[1]); }
#pragma unroll
    for (int a = 0; a < 6; ++a) { auto r = __builtin_amdgcn_permlane16_swap(__float_as_uint(a12[a]), __float_as_uint(a12[a + 6]), false, false); a6[a] = __uint_as_float(r[0]) + __uint_as_float(r[1]); }
#pragma unroll
    for (int a = 0; a < 3; ++a) { const float t1 = a6[a] + dpp<XOR8>(a6[a]), t2 = a6[a + 3] + dpp<XOR8>(a6[a + 3]); a3[a] = (lane & 8) ? t2 : t1; }
    const int ib = ((lane >> 5) & 1) * 12 + ((lane >> 4) & 1) * 6 + ((lane >> 3) & 1) * 3;
#pragma unroll
    for (int a = 0; a < 3; ++a) { const int idx = ib + a; op[w * 192 + (idx >> 3) * 64 + c * 8 + (idx & 7)] = a3[a]; }
    if (w == 0) STAMP(3);
    __syncthreads();
    __syncthreads();
    if (w == 0) STAMP(4);
    asm volatile("s_waitcnt vmcnt(0)" : "+v"(wd[0]), "+v"(wd[1]), "+v"(wd[2]), "+v"(wd[3]) :: "memory");
    {
      float ac[8];
#pragma unroll
      for (int e = 0; e < 8; ++e) ac[e] = 0.f;
#pragma unroll
      for (int j = 0; j < 4; ++j) { const float ov = os[rgD + 48 * j];
#pragma unroll
        for (int e = 0; e < 4; ++e) { ac[2 * e] = fmaf(ov, bflo(wd[j][e]), ac[2 * e]); ac[2 * e + 1] = fmaf(ov, bfhi(wd[j][e]), ac[2 * e + 1]); } }
      float* dst = red + (act ? rgD * 80 + cD * 8 : 48 * 80 + (t - 480) * 8);
      *reinterpret_cast<f32x4*>(dst) = f32x4{ac[0], ac[1], ac[2], ac[3]};
      *reinterpret_cast<f32x4*>(dst + 4) = f32x4{ac[4], ac[5], ac[6], ac[7]};
    }
    if (w == 0) STAMP(5);
    __syncthreads();
  } else {
    __builtin_amdgcn_s_setprio(3);
    const int ct = t - 512;
    if (ct == 0 && real) tagw = (__hip_atomic_load(ws + WS_EPOCH, RLX_AGENT) + 1ull);
    if (w == 8) STAMP(6);
    __syncthreads();
    __syncthreads();
    const u64 ep1 = tagw, tag = ep1 & 0xffffffffffull;
    u64 gq[8];
    if (ct < 192) {
      float rv[20];
#pragma unroll
      for (int r = 0; r < 20; ++r) rv[r] = red[r * 192 + ct];
      float a0 = 0.f, a1 = 0.f, a2 = 0.f, a3 = 0.f;
#pragma unroll
      for (int r = 0; r < 20; r += 4) { a0 += rv[r]; a1 += rv[r + 1]; a2 += rv[r + 2]; a3 += rv[r + 3]; }
      const float a = (a0 + a1) + (a2 + a3);
      if (real) put_g(ws + WS_H1 + (h * 8 + s) * 192 + ct, tag, a);
      else gq[0] = __float_as_uint(a);
      if (w == 8) STAMP(7);
    }
    __syncthreads();
    __syncthreads();
    if (ct < 192 && real) {
#pragma unroll
      for (int r = 0; r < 8; ++r) gq[r] = __hip_atomic_load(ws + WS_H1 + (h * 8 + r) * 192 + ct, RLX_AGENT);
    }
    __syncthreads();
    if (ct < 192) {
      for (unsigned spins = 0; spins < SPIN_CAP; ++spins) {
        bool ok = true;
#pragma unroll
        for (int r = 0; r < 8; ++r) ok &= chk_g(gq[r], tag);
        if (__all(ok) || !real) break;
#pragma unroll
        for (int r = 0; r < 8; ++r) gq[r] = __hip_atomic_load(ws + WS_H1 + (h * 8 + r) * 192 + ct, RLX_AGENT);
      }
      if (w == 8) STAMP(12);
      float q = 0.f;
#pragma unroll
      for (int r = 0; r < 8; ++r) q += val_g(gq[r]);
      qs[ct] = (uint16_t)(cvtpk(q, q) & 0xffffu);
    }
    __syncthreads();
    __syncthreads();
    if (w == 8) STAMP(19);
    __syncthreads();
    if (w == 8) STAMP(20);
    {
      gu64* dst = ws + WS_H2 + (h * 8 + s) * 200;
      float pv;
      if (ct < 192) {
        float ov[8];
#pragma unroll
        for (int r = 0; r < 8; ++r) ov[r] = op[r * 192 + ct];
        pv = ((ov[0] + ov[1]) + (ov[2] + ov[3])) + ((ov[4] + ov[5]) + (ov[6] + ov[7]));
      } else {
        const f32x4 m0 = *reinterpret_cast<const f32x4*>(mw), m1 = *reinterpret_cast<const f32x4*>(mw + 4);
        const f32x4 l0 = *reinterpret_cast<const f32x4*>(lw), l1 = *reinterpret_cast<const f32x4*>(lw + 4);
        const float M = fmaxf(fmaxf(fmaxf(m0[0], m0[1]), fmaxf(m0[2], m0[3])), fmaxf(fmaxf(m1[0], m1[1]), fmaxf(m1[2], m1[3])));
        const float L = ((l0[0] + l0[1]) + (l0[2] + l0[3])) + ((l1[0] + l1[1]) + (l1[2] + l1[3]));
        pv = ct == 192 ? M : L;
      }
      if (real) { if (ct < 194) put_g(dst + ct, tag, pv); }
      else os[ct & 127] = pv;
    }
    if (w == 8) STAMP(10);
    if (ct < 192) {
      u64 go[8], gs = 0;
#pragma unroll
      for (int r = 0; r < 8; ++r) go[r] = 0;
      const int sr = (lane >> 1) & 7, sw = lane & 1;
      for (unsigned spins = 0; spins < SPIN_CAP; ++spins) {
        if (real) {
          gs = __hip_atomic_load(ws + WS_H2 + (h * 8 + sr) * 200 + 192 + sw, RLX_AGENT);
#pragma unroll
          for (int r = 0; r < 8; ++r) go[r] = __hip_atomic_load(ws + WS_H2 + (h * 8 + r) * 200 + ct, RLX_AGENT);
        }
        bool ok = chk_g(gs, tag);
#pragma unroll
        for (int r = 0; r < 8; ++r) ok &= chk_g(go[r], tag);
        if (__all(ok) || !real) break;
      }
      if (w == 8) STAMP(11);
      const float sv = val_g(gs);
      float mr[8], lr[8];
#pragma unroll
      for (int r = 0; r < 8; ++r) { mr[r] = __builtin_bit_cast(float, __builtin_amdgcn_readlane(__builtin_bit_cast(int, sv), 2 * r)); lr[r] = __builtin_bit_cast(float, __builtin_amdgcn_readlane(__builtin_bit_cast(int, sv), 2 * r + 1)); }
      float M = mr[0];
#pragma unroll
      for (int r = 1; r < 8; ++r) M = fmaxf(M, mr[r]);
      float L = 0.f, o = 0.f;
#pragma unroll
      for (int r = 0; r < 8; ++r) { const float f = __builtin_amdgcn_exp2f((mr[r] - M) * LOG2E); L = fmaf(f, lr[r], L); o = fmaf(f, val_g(go[r]), o); }
      os[ct] = bfround(o / L);
    }
    __syncthreads();
    __syncthreads();
    if (ct < 80) {
      float rv[48];
#pragma unroll
      for (int r = 0; r < 48; ++r) rv[r] = red[r * 80 + ct];
      float a0 = 0.f, a1 = 0.f, a2 = 0.f, a3 = 0.f;
#pragma unroll
      for (int r = 0; r < 48; r += 4) { a0 += rv[r]; a1 += rv[r + 1]; a2 += rv[r + 2]; a3 += rv[r + 3]; }
      const float a = (a0 + a1) + (a2 + a3);
      if (real) put_g(ws + WS_H3 + (s * 32 + h) * 80 + ct, tag, a);
      else os[ct] = a;
    }
    if (w == 8) STAMP(8);
    if ((h < 10 || !real) && w == 11) {
      const int r0 = lane >> 3, col = lane & 7;
      u64 gf[4] = {0, 0, 0, 0};
      for (unsigned spins = 0; spins < SPIN_CAP; ++spins) {
        if (real) {
#pragma unroll
          for (int k = 0; k < 4; ++k) gf[k] = __hip_atomic_load(ws + WS_H3 + (s * 32 + r0 + 8 * k) * 80 + h * 8 + col, RLX_AGENT);
        }
        bool ok = true;
#pragma unroll
        for (int k = 0; k < 4; ++k) ok &= chk_g(gf[k], tag);
        if (__all(ok) || !real) break;
      }
      float a = (val_g(gf[0]) + val_g(gf[1])) + (val_g(gf[2]) + val_g(gf[3]));
      a += dpp<XOR8>(a);
      a = x16_sum(a);
      const unsigned ob = cvtpk(a, a) & 0xffffu;
      if (real) {
        if (lane < 8) out[s * 80 + h * 8 + lane] = (uint16_t)ob;
        STAMP(9);
        if (b == 0 && lane == 0) __hip_atomic_store(ws + WS_EPOCH, ep1, RLX_AGENT);
      } else qs[lane] = (uint16_t)ob;
    }
    __builtin_amdgcn_s_waitcnt(0);
  }
  STAMP_DUMP;
}

extern "C" void kernel_launch(void* const* d_in, const int* in_sizes, int n_in, void* d_out, int out_size, void* d_ws, size_t ws_size, hipStream_t stream) {
  fused<<<256 + NDRY, 768, 0, stream>>>((const uint16_t*)d_in[0], (const uint16_t*)d_in[1], (const uint16_t*)d_in[2], (const uint16_t*)d_in[3], (const uint16_t*)d_in[4],
                                 (uint16_t*)d_out, (u64*)d_ws);
}
```

```cpp
#define PROBE 0
#include <hip/hip_runtime.h>
#include <hip/hip_bf16.h>
#include <stdint.h>

typedef unsigned u32x4 __attribute__((ext_vector_type(4)));
typedef float f32x4 __attribute__((ext_vector_type(4)));
typedef __bf16 bf16x2_t __attribute__((ext_vector_type(2)));
typedef unsigned long long u64;
typedef __attribute__((address_space(1))) u64 gu64;

constexpr int DM = 640, NH = 32, SK = 4096, DH = 192, WIN = 1024, NS = 8;
constexpr int HD = NH * DH;
#define CODEPF 0
#define TLBPF 0
#define NWA 4
#ifndef NV1
#define NV1 2
#endif
constexpr unsigned SPIN_CAP = 1u << 18;

constexpr int WS_EPOCH = 0;
constexpr int WS_H1 = 64;
constexpr int WS_H2 = WS_H1 + NH * NS * 192;
constexpr int WS_H3 = WS_H2 + NH * NS * 200;

__device__ __forceinline__ float bflo(unsigned u) { return __uint_as_float(u << 16); }
__device__ __forceinline__ float bfhi(unsigned u) { return __uint_as_float(u & 0xffff0000u); }
__device__ __forceinline__ float dot2(unsigned k, unsigned q, float acc) {
  return __builtin_amdgcn_fdot2_f32_bf16(__builtin_bit_cast(bf16x2_t, k), __builtin_bit_cast(bf16x2_t, q), acc, false);
}
__device__ __forceinline__ unsigned cvtpk(float lo, float hi) {
  unsigned r; asm volatile("v_cvt_pk_bf16_f32 %0, %1, %2" : "=v"(r) : "v"(lo), "v"(hi)); return r;
}
__device__ __forceinline__ float bfround(float x) { return bflo(cvtpk(x, x)); }
template <int CTRL> __device__ __forceinline__ float dpp(float x) {
  return __builtin_bit_cast(float, __builtin_amdgcn_mov_dpp(__builtin_bit_cast(int, x), CTRL, 0xf, 0xf, true));
}
constexpr int XOR1 = 0xB1, XOR2 = 0x4E, XOR7 = 0x141, XOR8 = 0x128;
__device__ __forceinline__ u32x4 ldnt(const void* p) { return __builtin_nontemporal_load(reinterpret_cast<const u32x4*>(p)); }
__device__ __forceinline__ float x16_sum(float l) {
  { auto r = __builtin_amdgcn_permlane16_swap(__float_as_uint(l), __float_as_uint(l), false, false); l = __uint_as_float(r[0]) + __uint_as_float(r[1]); }
  { auto r = __builtin_amdgcn_permlane32_swap(__float_as_uint(l), __float_as_uint(l), false, false); l = __uint_as_float(r[0]) + __uint_as_float(r[1]); }
  return l;
}
__device__ __forceinline__ float x16_max(float m) {
  { auto r = __builtin_amdgcn_permlane16_swap(__float_as_uint(m), __float_as_uint(m), false, false); m = fmaxf(__uint_as_float(r[0]), __uint_as_float(r[1])); }
  { auto r = __builtin_amdgcn_permlane32_swap(__float_as_uint(m), __float_as_uint(m), false, false); m = fmaxf(__uint_as_float(r[0]), __uint_as_float(r[1])); }
  return m;
}
#define RLX_AGENT __ATOMIC_RELAXED, __HIP_MEMORY_SCOPE_AGENT
__device__ __forceinline__ void put_g(gu64* g, u64 tag, float v) {
  const unsigned hi = ((__float_as_uint(v) + 0x80u) & 0xffffff00u) | (unsigned)(tag >> 32);
  __hip_atomic_store(g, ((u64)hi << 32) | (unsigned)tag, RLX_AGENT);
}
__device__ __forceinline__ bool chk_g(u64 x, u64 tag) { return (x & 0xffffffffffull) == tag; }
__device__ __forceinline__ float val_g(u64 x) { return __uint_as_float((unsigned)(x >> 32) & 0xffffff00u); }

#define STAMP(i) do { if ((threadIdx.x & 63) == 0) stamps[i] = __builtin_amdgcn_s_memrealtime(); } while (0)
#define STAMP_DECL __shared__ u64 stamps[32];
#define STAMP_DUMP
constexpr float LOG2E = 1.4426950408889634f;
#define SB() __builtin_amdgcn_sched_barrier(0)

__global__ void __launch_bounds__(768) fused(const uint16_t* __restrict__ x, const uint16_t* __restrict__ kc, const uint16_t* __restrict__ vc,
                                             const uint16_t* __restrict__ wq, const uint16_t* __restrict__ wo, uint16_t* __restrict__ out, u64* ws_) {
  __shared__ __attribute__((aligned(16))) float red[48 * 80 + 32 * 8];
  __shared__ __attribute__((aligned(16))) float op[8 * 192];
  __shared__ __attribute__((aligned(16))) uint16_t qs[192];
  __shared__ __attribute__((aligned(16))) float os[192];
  __shared__ __attribute__((aligned(16))) float mw[8];
  __shared__ __attribute__((aligned(16))) float lw[8];
  __shared__ u64 tagw;
  STAMP_DECL
  u64 pc0; asm volatile("s_getpc_b64 %0" : "=s"(pc0));
  gu64* ws = (gu64*)ws_;
  const int b = blockIdx.x, jj = b >> 3;
  const int h = (b & 7) * 4 + (jj >> 3), s = jj & 7;
  const int t = threadIdx.x, w = t >> 6, lane = t & 63;
  if (w < 8) {
    if (w == 0) STAMP(0);
    const int tc = t < 480 ? t : t - 32;
    const int cA = tc % 24, rgA = tc / 24, cD = tc % 10, rgD = tc / 10;
    const int g = lane >> 3, c = lane & 7;
    u32x4 wa[4], K[2][3], V[2][3], wd[4]; uint16_t xr[4];
    const bool act = t < 480;
#pragma unroll
    for (int j = 0; j < 4; ++j) xr[j] = x[s * 80 + rgA + 20 * j];
#pragma unroll
    for (int j = 0; j < 4; ++j) wa[j] = ldnt(wq + (size_t)(s * 80 + rgA + 20 * j) * HD + h * DH + cA * 8);
    SB();
    __syncthreads();
    const size_t base = ((size_t)h * SK + (SK - WIN) + s * 128 + w * 16 + g) * DH + c * 8;
#pragma unroll
    for (int j = 0; j < 2; ++j)
#pragma unroll
      for (int u = 0; u < 3; ++u) K[j][u] = ldnt(kc + base + j * 8 * DH + u * 64);
    SB();
    {
      float acc[8];
#pragma unroll
      for (int e = 0; e < 8; ++e) acc[e] = 0.f;
#pragma unroll
      for (int j = 0; j < 4; ++j) { const float xv = bflo(xr[j]);
#pragma unroll
        for (int e = 0; e < 4; ++e) { acc[2 * e] = fmaf(xv, bflo(wa[j][e]), acc[2 * e]); acc[2 * e + 1] = fmaf(xv, bfhi(wa[j][e]), acc[2 * e + 1]); } }
      float* dst = red + (act ? rgA * 192 + cA * 8 : 48 * 80 + (t - 480) * 8);
      *reinterpret_cast<f32x4*>(dst) = f32x4{acc[0], acc[1], acc[2], acc[3]};
      *reinterpret_cast<f32x4*>(dst + 4) = f32x4{acc[4], acc[5], acc[6], acc[7]};
    }
    if (w == 0) STAMP(1);
    __syncthreads();
    __syncthreads();
#pragma unroll
    for (int i = 0; i < NV1; ++i) V[i / 3][i % 3] = ldnt(vc + base + (i / 3) * 8 * DH + (i % 3) * 64);
    SB();
    __syncthreads();
    __syncthreads();
#pragma unroll
    for (int i = NV1; i < 6; ++i) V[i / 3][i % 3] = ldnt(vc + base + (i / 3) * 8 * DH + (i % 3) * 64);
    SB();
    if (w == 0) STAMP(16);
    if (w == 7) STAMP(17);
    if (w == 3) STAMP(18);
    __syncthreads();
    if (w == 0) STAMP(2);
    u32x4 qf[3];
#pragma unroll
    for (int u = 0; u < 3; ++u) qf[u] = *reinterpret_cast<const u32x4*>(qs + u * 64 + c * 8);
    float sc[2];
#pragma unroll
    for (int j = 0; j < 2; ++j) {
      float d0 = 0.f, d1 = 0.f, d2 = 0.f;
#pragma unroll
      for (int e = 0; e < 4; ++e) { d0 = dot2(K[j][0][e], qf[0][e], d0); d1 = dot2(K[j][1][e], qf[1][e], d1); d2 = dot2(K[j][2][e], qf[2][e], d2); }
      float d = (d0 + d1) + d2;
      d += dpp<XOR1>(d); d += dpp<XOR2>(d); d += dpp<XOR7>(d);
      sc[j] = bfround(bfround(d) * 0.07216878364870322f);
    }
    float m = fmaxf(sc[0], sc[1]);
    m = fmaxf(m, dpp<XOR8>(m));
    m = x16_max(m);
    if (lane == 0) mw[w] = m;
    __syncthreads();
    {
      const f32x4 m0 = *reinterpret_cast<const f32x4*>(mw), m1 = *reinterpret_cast<const f32x4*>(mw + 4);
      m = fmaxf(fmaxf(fmaxf(m0[0], m0[1]), fmaxf(m0[2], m0[3])), fmaxf(fmaxf(m1[0], m1[1]), fmaxf(m1[2], m1[3])));
    }
    const float p0 = __builtin_amdgcn_exp2f((sc[0] - m) * LOG2E), p1 = __builtin_amdgcn_exp2f((sc[1] - m) * LOG2E);
    float l = p0 + p1;
    l += dpp<XOR8>(l);
    l = x16_sum(l);
    if (lane == 0) lw[w] = l;
    SB();
#pragma unroll
    for (int j = 0; j < NWA; ++j) wd[j] = ldnt(wo + (size_t)(h * DH + rgD + 48 * j) * DM + s * 80 + cD * 8);
    SB();
    float acc[24];
#pragma unroll
    for (int u = 0; u < 3; ++u)
#pragma unroll
      for (int e = 0; e < 4; ++e) {
        acc[u * 8 + 2 * e] = fmaf(p1, bflo(V[1][u][e]), p0 * bflo(V[0][u][e]));
        acc[u * 8 + 2 * e + 1] = fmaf(p1, bfhi(V[1][u][e]), p0 * bfhi(V[0][u][e]));
      }
    float a12[12], a6[6], a3[3];
#pragma unroll
    for (int a = 0; a < 12; ++a) { auto r = __builtin_amdgcn_permlane32_swap(__float_as_uint(acc[a]), __float_as_uint(acc[a + 12]), false, false); a12[a] = __uint_as_float(r[0]) + __uint_as_float(r[1]); }
#pragma unroll
    for (int a = 0; a < 6; ++a) { auto r = __builtin_amdgcn_permlane16_swap(__float_as_uint(a12[a]), __float_as_uint(a12[a + 6]), false, false); a6[a] = __uint_as_float(r[0]) + __uint_as_float(r[1]); }
#pragma unroll
    for (int a = 0; a < 3; ++a) { const float t1 = a6[a] + dpp<XOR8>(a6[a]), t2 = a6[a + 3] + dpp<XOR8>(a6[a + 3]); a3[a] = (lane & 8) ? t2 : t1; }
    const int ib = ((lane >> 5) & 1) * 12 + ((lane >> 4) & 1) * 6 + ((lane >> 3) & 1) * 3;
#pragma unroll
    for (int a = 0; a < 3; ++a) { const int idx = ib + a; op[w * 192 + (idx >> 3) * 64 + c * 8 + (idx & 7)] = a3[a]; }
    if (w == 0) STAMP(3);

    __syncthreads();
    __syncthreads();
#pragma unroll
    for (int j = NWA; j < 4; ++j) wd[j] = ldnt(wo + (size_t)(h * DH + rgD + 48 * j) * DM + s * 80 + cD * 8);
    SB();
    __syncthreads();
    __syncthreads();
    if (w == 0) STAMP(4);
    {
      float ac[8];
#pragma unroll
      for (int e = 0; e < 8; ++e) ac[e] = 0.f;
#pragma unroll
      for (int j = 0; j < 4; ++j) { const float ov = os[rgD + 48 * j];
#pragma unroll
        for (int e = 0; e < 4; ++e) { ac[2 * e] = fmaf(ov, bflo(wd[j][e]), ac[2 * e]); ac[2 * e + 1] = fmaf(ov, bfhi(wd[j][e]), ac[2 * e + 1]); } }
      float* dst = red + (act ? rgD * 80 + cD * 8 : 48 * 80 + (t - 480) * 8);
      *reinterpret_cast<f32x4*>(dst) = f32x4{ac[0], ac[1], ac[2], ac[3]};
      *reinterpret_cast<f32x4*>(dst + 4) = f32x4{ac[4], ac[5], ac[6], ac[7]};
    }
    if (w == 0) STAMP(5);
    if (w == 7) STAMP(22);
    if (w == 3) STAMP(23);
    __syncthreads();
  } else {
    __builtin_amdgcn_s_setprio(3);
    const int ct = t - 512;
    unsigned pfa = 0, pfb = 0; u32x4 pfv[9];
#pragma unroll
    for (int i = 0; i < 9; ++i) pfv[i] = u32x4{0u, 0u, 0u, 0u};
    if (ct == 0) tagw = (__hip_atomic_load(ws + WS_EPOCH, RLX_AGENT) + 1ull);
    if (w == 8) STAMP(6);
    __syncthreads();
    __syncthreads();
    asm volatile("" :: "v"(pfa), "v"(pfb));
#pragma unroll
    for (int i = 0; i < 9; ++i) asm volatile("" :: "v"(pfv[i]));
    const u64 ep1 = tagw, tag = ep1 & 0xffffffffffull;
    u64 gq[8];
    if (ct < 192) {
      float rv[20];
#pragma unroll
      for (int r = 0; r < 20; ++r) rv[r] = red[r * 192 + ct];
      float a0 = 0.f, a1 = 0.f, a2 = 0.f, a3 = 0.f;
#pragma unroll
      for (int r = 0; r < 20; r += 4) { a0 += rv[r]; a1 += rv[r + 1]; a2 += rv[r + 2]; a3 += rv[r + 3]; }
      put_g(ws + WS_H1 + (h * 8 + s) * 192 + ct, tag, (a0 + a1) + (a2 + a3));
      if (w == 8) STAMP(7);
    }
    __syncthreads();
    __syncthreads();
    if (ct < 192) {
#pragma unroll
      for (int r = 0; r < 8; ++r) gq[r] = __hip_atomic_load(ws + WS_H1 + (h * 8 + r) * 192 + ct, RLX_AGENT);
    }
    __syncthreads();
    if (ct < 192) {
      for (unsigned spins = 0; spins < SPIN_CAP; ++spins) {
        bool ok = true;
#pragma unroll
        for (int r = 0; r < 8; ++r) ok &= chk_g(gq[r], tag);
        if (__all(ok)) break;
        if (w == 8) STAMP(13);
#pragma unroll
        for (int r = 0; r < 8; ++r) gq[r] = __hip_atomic_load(ws + WS_H1 + (h * 8 + r) * 192 + ct, RLX_AGENT);
      }
      if (w == 8) STAMP(12);
      if (w == 9) STAMP(14);
      if (w == 10) STAMP(15);
      float q = 0.f;
#pragma unroll
      for (int r = 0; r < 8; ++r) q += val_g(gq[r]);
      qs[ct] = (uint16_t)(cvtpk(q, q) & 0xffffu);
    }
    __syncthreads();
    __syncthreads();
    if (w == 8) STAMP(19);
    __syncthreads();
    if (w == 8) STAMP(20);
    {
      gu64* dst = ws + WS_H2 + (h * 8 + s) * 200;
      if (ct < 192) {
        float ov[8];
#pragma unroll
        for (int r = 0; r < 8; ++r) ov[r] = op[r * 192 + ct];
        put_g(dst + ct, tag, ((ov[0] + ov[1]) + (ov[2] + ov[3])) + ((ov[4] + ov[5]) + (ov[6] + ov[7])));
      } else if (ct < 194) {
        const f32x4 m0 = *reinterpret_cast<const f32x4*>(mw), m1 = *reinterpret_cast<const f32x4*>(mw + 4);
        const f32x4 l0 = *reinterpret_cast<const f32x4*>(lw), l1 = *reinterpret_cast<const f32x4*>(lw + 4);
        const float M = fmaxf(fmaxf(fmaxf(m0[0], m0[1]), fmaxf(m0[2], m0[3])), fmaxf(fmaxf(m1[0], m1[1]), fmaxf(m1[2], m1[3])));
        const float L = ((l0[0] + l0[1]) + (l0[2] + l0[3])) + ((l1[0] + l1[1]) + (l1[2] + l1[3]));
        put_g(dst + ct, tag, ct == 192 ? M : L);
      }
    }
    if (w == 8) STAMP(10);
    __syncthreads();
    __syncthreads();
    if (ct < 192) {
      u64 go[8], gs;
      const int sr = (lane >> 1) & 7, sw = lane & 1;
      for (unsigned spins = 0; spins < SPIN_CAP; ++spins) {
        bool ok = true;
        gs = __hip_atomic_load(ws + WS_H2 + (h * 8 + sr) * 200 + 192 + sw, RLX_AGENT); ok &= chk_g(gs, tag);
#pragma unroll
        for (int r = 0; r < 8; ++r) { go[r] = __hip_atomic_load(ws + WS_H2 + (h * 8 + r) * 200 + ct, RLX_AGENT); ok &= chk_g(go[r], tag); }
        if (__all(ok)) break;
        __builtin_amdgcn_s_sleep(1);
      }
      if (w == 8) STAMP(11);
      const float sv = val_g(gs);
      float mr[8], lr[8];
#pragma unroll
      for (int r = 0; r < 8; ++r) { mr[r] = __builtin_bit_cast(float, __builtin_amdgcn_readlane(__builtin_bit_cast(int, sv), 2 * r)); lr[r] = __builtin_bit_cast(float, __builtin_amdgcn_readlane(__builtin_bit_cast(int, sv), 2 * r + 1)); }
      float M = mr[0];
#pragma unroll
      for (int r = 1; r < 8; ++r) M = fmaxf(M, mr[r]);
      float L = 0.f, o = 0.f;
#pragma unroll
      for (int r = 0; r < 8; ++r) { const float f = __builtin_amdgcn_exp2f((mr[r] - M) * LOG2E); L = fmaf(f, lr[r], L); o = fmaf(f, val_g(go[r]), o); }
      os[ct] = bfround(o / L);
    }
    __syncthreads();
    __syncthreads();
    if (w == 8) STAMP(21);
    if (ct < 80) {
      float rv[48];
#pragma unroll
      for (int r = 0; r < 48; ++r) rv[r] = red[r * 80 + ct];
      float a0 = 0.f, a1 = 0.f, a2 = 0.f, a3 = 0.f;
#pragma unroll
      for (int r = 0; r < 48; r += 4) { a0 += rv[r]; a1 += rv[r + 1]; a2 += rv[r + 2]; a3 += rv[r + 3]; }
      put_g(ws + WS_H3 + (s * 32 + h) * 80 + ct, tag, (a0 + a1) + (a2 + a3));
    }
    if (w == 8) STAMP(8);
    if (h < 10 && w == 11) {
      const int r0 = lane >> 3, col = lane & 7;
      u64 gf[4];
      for (unsigned spins = 0; spins < SPIN_CAP; ++spins) {
        bool ok = true;
#pragma unroll
        for (int k = 0; k < 4; ++k) { gf[k] = __hip_atomic_load(ws + WS_H3 + (s * 32 + r0 + 8 * k) * 80 + h * 8 + col, RLX_AGENT); ok &= chk_g(gf[k], tag); }
        if (__all(ok)) break;
        __builtin_amdgcn_s_sleep(1);
      }
      float a = (val_g(gf[0]) + val_g(gf[1])) + (val_g(gf[2]) + val_g(gf[3]));
      a += dpp<XOR8>(a);
      a = x16_sum(a);
      if (lane < 8) out[s * 80 + h * 8 + lane] = (uint16_t)(cvtpk(a, a) & 0xffffu);
      STAMP(9);
      if (b == 0 && lane == 0) __hip_atomic_store(ws + WS_EPOCH, ep1, RLX_AGENT);
      { const volatile u64* vs = stamps; const u64 d = vs[PROBE] - vs[0]; const u64 tgt = __builtin_amdgcn_s_memrealtime() + 4 * (d > 5000 ? 0 : d);
        for (int i = 0; i < 1000000; ++i) { if (__builtin_amdgcn_s_memrealtime() >= tgt) break; __builtin_amdgcn_s_sleep(1); } }
    }
  }
  STAMP_DUMP;
}

extern "C" void kernel_launch(void* const* d_in, const int* in_sizes, int n_in, void* d_out, int out_size, void* d_ws, size_t ws_size, hipStream_t stream) {
  fused<<<256, 768, 0, stream>>>((const uint16_t*)d_in[0], (const uint16_t*)d_in[1], (const uint16_t*)d_in[2], (const uint16_t*)d_in[3], (const uint16_t*)d_in[4],
                                 (uint16_t*)d_out, (u64*)d_ws);
}
```

```cpp
#include <hip/hip_runtime.h>
#include <hip/hip_bf16.h>
#include <stdint.h>

typedef unsigned u32x4 __attribute__((ext_vector_type(4)));
typedef float f32x4 __attribute__((ext_vector_type(4)));
typedef __bf16 bf16x2_t __attribute__((ext_vector_type(2)));
typedef unsigned long long u64;
typedef __attribute__((address_space(1))) u64 gu64;

constexpr int DM = 640, NH = 32, SK = 4096, DH = 192, WIN = 1024, NS = 8;
constexpr int HD = NH * DH;
#define CODEPF 64
#define TLBPF 0
#define NKE 4
#define NWA 4
#ifndef NV1
#define NV1 2
#endif
constexpr unsigned SPIN_CAP = 1u << 18;

constexpr int WS_EPOCH = 0;
constexpr int WS_H1 = 64;
constexpr int WS_H2 = WS_H1 + NH * NS * 192;
constexpr int WS_H3 = WS_H2 + NH * NS * 200;

__device__ __forceinline__ float bflo(unsigned u) { return __uint_as_float(u << 16); }
__device__ __forceinline__ float bfhi(unsigned u) { return __uint_as_float(u & 0xffff0000u); }
__device__ __forceinline__ float dot2(unsigned k, unsigned q, float acc) {
  return __builtin_amdgcn_fdot2_f32_bf16(__builtin_bit_cast(bf16x2_t, k), __builtin_bit_cast(bf16x2_t, q), acc, false);
}
__device__ __forceinline__ unsigned cvtpk(float lo, float hi) {
  unsigned r; asm volatile("v_cvt_pk_bf16_f32 %0, %1, %2" : "=v"(r) : "v"(lo), "v"(hi)); return r;
}
__device__ __forceinline__ float bfround(float x) { return bflo(cvtpk(x, x)); }
template <int CTRL> __device__ __forceinline__ float dpp(float x) {
  return __builtin_bit_cast(float, __builtin_amdgcn_mov_dpp(__builtin_bit_cast(int, x), CTRL, 0xf, 0xf, true));
}
constexpr int XOR1 = 0xB1, XOR2 = 0x4E, XOR7 = 0x141, XOR8 = 0x128;
__device__ __forceinline__ u32x4 ldnt(const void* p) { return __builtin_nontemporal_load(reinterpret_cast<const u32x4*>(p)); }
__device__ __forceinline__ float x16_sum(float l) {
  { auto r = __builtin_amdgcn_permlane16_swap(__float_as_uint(l), __float_as_uint(l), false, false); l = __uint_as_float(r[0]) + __uint_as_float(r[1]); }
  { auto r = __builtin_amdgcn_permlane32_swap(__float_as_uint(l), __float_as_uint(l), false, false); l = __uint_as_float(r[0]) + __uint_as_float(r[1]); }
  return l;
}
__device__ __forceinline__ float x16_max(float m) {
  { auto r = __builtin_amdgcn_permlane16_swap(__float_as_uint(m), __float_as_uint(m), false, false); m = fmaxf(__uint_as_float(r[0]), __uint_as_float(r[1])); }
  { auto r = __builtin_amdgcn_permlane32_swap(__float_as_uint(m), __float_as_uint(m), false, false); m = fmaxf(__uint_as_float(r[0]), __uint_as_float(r[1])); }
  return m;
}
#define RLX_AGENT __ATOMIC_RELAXED, __HIP_MEMORY_SCOPE_AGENT
__device__ __forceinline__ void put_g(gu64* g, u64 tag, float v) {
  const unsigned hi = ((__float_as_uint(v) + 0x80u) & 0xffffff00u) | (unsigned)(tag >> 32);
  __hip_atomic_store(g, ((u64)hi << 32) | (unsigned)tag, RLX_AGENT);
}
__device__ __forceinline__ bool chk_g(u64 x, u64 tag) { return (x & 0xffffffffffull) == tag; }
__device__ __forceinline__ float val_g(u64 x) { return __uint_as_float((unsigned)(x >> 32) & 0xffffff00u); }

#define STAMP(i)
#define STAMP_DECL
#define STAMP_DUMP
constexpr float LOG2E = 1.4426950408889634f;
#define SB() __builtin_amdgcn_sched_barrier(0)

__global__ void __launch_bounds__(768) fused(const uint16_t* __restrict__ x, const uint16_t* __restrict__ kc, const uint16_t* __restrict__ vc,
                                             const uint16_t* __restrict__ wq, const uint16_t* __restrict__ wo, uint16_t* __restrict__ out, u64* ws_) {
  __shared__ __attribute__((aligned(16))) float red[48 * 80 + 32 * 8];
  __shared__ __attribute__((aligned(16))) float op[8 * 192];
  __shared__ __attribute__((aligned(16))) uint16_t qs[192];
  __shared__ __attribute__((aligned(16))) float os[192];
  __shared__ __attribute__((aligned(16))) float mw[8];
  __shared__ __attribute__((aligned(16))) float lw[8];
  __shared__ u64 tagw;
  STAMP_DECL
  u64 pc0; asm volatile("s_getpc_b64 %0" : "=s"(pc0));
  gu64* ws = (gu64*)ws_;
  const int b = blockIdx.x, jj = b >> 3;
  const int h = (b & 7) * 4 + (jj >> 3), s = jj & 7;
  const int t = threadIdx.x, w = t >> 6, lane = t & 63;
  if (w < 8) {
    if (w == 0) STAMP(0);
    const int tc = t < 480 ? t : t - 32;
    const int cA = tc % 24, rgA = tc / 24, cD = tc % 10, rgD = tc / 10;
    const int g = lane >> 3, c = lane & 7;
    u32x4 wa[4], K[2][3], V[2][3], wd[4]; uint16_t xr[4];
    const bool act = t < 480;
#pragma unroll
    for (int j = 0; j < 4; ++j) xr[j] = x[s * 80 + rgA + 20 * j];
#pragma unroll
    for (int j = 0; j < 4; ++j) wa[j] = ldnt(wq + (size_t)(s * 80 + rgA + 20 * j) * HD + h * DH + cA * 8);
    SB();
    __syncthreads();
    const size_t base = ((size_t)h * SK + (SK - WIN) + s * 128 + w * 16 + g) * DH + c * 8;
#pragma unroll
    for (int i = 0; i < NKE; ++i) K[i / 3][i % 3] = ldnt(kc + base + (i / 3) * 8 * DH + (i % 3) * 64);
    SB();
    {
      float acc[8];
#pragma unroll
      for (int e = 0; e < 8; ++e) acc[e] = 0.f;
#pragma unroll
      for (int j = 0; j < 4; ++j) { const float xv = bflo(xr[j]);
#pragma unroll
        for (int e = 0; e < 4; ++e) { acc[2 * e] = fmaf(xv, bflo(wa[j][e]), acc[2 * e]); acc[2 * e + 1] = fmaf(xv, bfhi(wa[j][e]), acc[2 * e + 1]); } }
      float* dst = red + (act ? rgA * 192 + cA * 8 : 48 * 80 + (t - 480) * 8);
      *reinterpret_cast<f32x4*>(dst) = f32x4{acc[0], acc[1], acc[2], acc[3]};
      *reinterpret_cast<f32x4*>(dst + 4) = f32x4{acc[4], acc[5], acc[6], acc[7]};
    }
    if (w == 0) STAMP(1);
    __syncthreads();
    __syncthreads();
#pragma unroll
    for (int i = NKE; i < 6; ++i) K[i / 3][i % 3] = ldnt(kc + base + (i / 3) * 8 * DH + (i % 3) * 64);
#pragma unroll
    for (int i = 0; i < NV1; ++i) V[i / 3][i % 3] = ldnt(vc + base + (i / 3) * 8 * DH + (i % 3) * 64);
    SB();
    __syncthreads();
    __syncthreads();
#pragma unroll
    for (int i = NV1; i < 6; ++i) V[i / 3][i % 3] = ldnt(vc + base + (i / 3) * 8 * DH + (i % 3) * 64);
    SB();
    if (w == 0) STAMP(16);
    if (w == 7) STAMP(17);
    if (w == 3) STAMP(18);
    __syncthreads();
    if (w == 0) STAMP(2);
    u32x4 qf[3];
#pragma unroll
    for (int u = 0; u < 3; ++u) qf[u] = *reinterpret_cast<const u32x4*>(qs + u * 64 + c * 8);
    float sc[2];
#pragma unroll
    for (int j = 0; j < 2; ++j) {
      float d0 = 0.f, d1 = 0.f, d2 = 0.f;
#pragma unroll
      for (int e = 0; e < 4; ++e) { d0 = dot2(K[j][0][e], qf[0][e], d0); d1 = dot2(K[j][1][e], qf[1][e], d1); d2 = dot2(K[j][2][e], qf[2][e], d2); }
      float d = (d0 + d1) + d2;
      d += dpp<XOR1>(d); d += dpp<XOR2>(d); d += dpp<XOR7>(d);
      sc[j] = bfround(bfround(d) * 0.07216878364870322f);
    }
    float m = fmaxf(sc[0], sc[1]);
    m = fmaxf(m, dpp<XOR8>(m));
    m = x16_max(m);
    if (lane == 0) mw[w] = m;
    __syncthreads();
    {
      const f32x4 m0 = *reinterpret_cast<const f32x4*>(mw), m1 = *reinterpret_cast<const f32x4*>(mw + 4);
      m = fmaxf(fmaxf(fmaxf(m0[0], m0[1]), fmaxf(m0[2], m0[3])), fmaxf(fmaxf(m1[0], m1[1]), fmaxf(m1[2], m1[3])));
    }
    const float p0 = __builtin_amdgcn_exp2f((sc[0] - m) * LOG2E), p1 = __builtin_amdgcn_exp2f((sc[1] - m) * LOG2E);
    float l = p0 + p1;
    l += dpp<XOR8>(l);
    l = x16_sum(l);
    if (lane == 0) lw[w] = l;
    SB();
#pragma unroll
    for (int j = 0; j < NWA; ++j) wd[j] = ldnt(wo + (size_t)(h * DH + rgD + 48 * j) * DM + s * 80 + cD * 8);
    SB();
    float acc[24];
#pragma unroll
    for (int u = 0; u < 3; ++u)
#pragma unroll
      for (int e = 0; e < 4; ++e) {
        acc[u * 8 + 2 * e] = fmaf(p1, bflo(V[1][u][e]), p0 * bflo(V[0][u][e]));
        acc[u * 8 + 2 * e + 1] = fmaf(p1, bfhi(V[1][u][e]), p0 * bfhi(V[0][u][e]));
      }
    float a12[12], a6[6], a3[3];
#pragma unroll
    for (int a = 0; a < 12; ++a) { auto r = __builtin_amdgcn_permlane32_swap(__float_as_uint(acc[a]), __float_as_uint(acc[a + 12]), false, false); a12[a] = __uint_as_float(r[0]) + __uint_as_float(r[1]); }
#pragma unroll
    for (int a = 0; a < 6; ++a) { auto r = __builtin_amdgcn_permlane16_swap(__float_as_uint(a12[a]), __float_as_uint(a12[a + 6]), false, false); a6[a] = __uint_as_float(r[0]) + __uint_as_float(r[1]); }
#pragma unroll
    for (int a = 0; a < 3; ++a) { const float t1 = a6[a] + dpp<XOR8>(a6[a]), t2 = a6[a + 3] + dpp<XOR8>(a6[a + 3]); a3[a] = (lane & 8) ? t2 : t1; }
    const int ib = ((lane >> 5) & 1) * 12 + ((lane >> 4) & 1) * 6 + ((lane >> 3) & 1) * 3;
#pragma unroll
    for (int a = 0; a < 3; ++a) { const int idx = ib + a; op[w * 192 + (idx >> 3) * 64 + c * 8 + (idx & 7)] = a3[a]; }
    if (w == 0) STAMP(3);
    if (w == 7) STAMP(21);
    if (w == 4) STAMP(22);
    __syncthreads();
    __syncthreads();
#pragma unroll
    for (int j = NWA; j < 4; ++j) wd[j] = ldnt(wo + (size_t)(h * DH + rgD + 48 * j) * DM + s * 80 + cD * 8);
    SB();
    __syncthreads();
    __syncthreads();
    if (w == 0) STAMP(4);
    {
      float ac[8];
#pragma unroll
      for (int e = 0; e < 8; ++e) ac[e] = 0.f;
#pragma unroll
      for (int j = 0; j < 4; ++j) { const float ov = os[rgD + 48 * j];
#pragma unroll
        for (int e = 0; e < 4; ++e) { ac[2 * e] = fmaf(ov, bflo(wd[j][e]), ac[2 * e]); ac[2 * e + 1] = fmaf(ov, bfhi(wd[j][e]), ac[2 * e + 1]); } }
      float* dst = red + (act ? rgD * 80 + cD * 8 : 48 * 80 + (t - 480) * 8);
      *reinterpret_cast<f32x4*>(dst) = f32x4{ac[0], ac[1], ac[2], ac[3]};
      *reinterpret_cast<f32x4*>(dst + 4) = f32x4{ac[4], ac[5], ac[6], ac[7]};
    }
    if (w == 0) STAMP(5);
    __syncthreads();
  } else {
    __builtin_amdgcn_s_setprio(3);
    const int ct = t - 512;
    unsigned pfa = 0, pfb = 0; u32x4 pfv[12];
#pragma unroll
    for (int i = 0; i < 12; ++i) pfv[i] = u32x4{0u, 0u, 0u, 0u};
    if (w == 11 && b < CODEPF) {
      const char* cb = (const char*)(pc0 & ~63ull);
#pragma unroll
      for (int i = 0; i < 12; ++i) pfv[i] = *reinterpret_cast<const __attribute__((address_space(1))) u32x4*>((const __attribute__((address_space(1))) char*)cb + i * 1024 + lane * 16);
    }
    if (ct == 0) tagw = (__hip_atomic_load(ws + WS_EPOCH, RLX_AGENT) + 1ull);
    if (w == 8) STAMP(6);
    __syncthreads();
    __syncthreads();
    asm volatile("" :: "v"(pfa), "v"(pfb));
#pragma unroll
    for (int i = 0; i < 12; ++i) asm volatile("" :: "v"(pfv[i]));
    const u64 ep1 = tagw, tag = ep1 & 0xffffffffffull;
    u64 gq[8];
    if (ct < 192) {
      float rv[20];
#pragma unroll
      for (int r = 0; r < 20; ++r) rv[r] = red[r * 192 + ct];
      float a0 = 0.f, a1 = 0.f, a2 = 0.f, a3 = 0.f;
#pragma unroll
      for (int r = 0; r < 20; r += 4) { a0 += rv[r]; a1 += rv[r + 1]; a2 += rv[r + 2]; a3 += rv[r + 3]; }
      put_g(ws + WS_H1 + (h * 8 + s) * 192 + ct, tag, (a0 + a1) + (a2 + a3));
      if (w == 8) STAMP(7);
    }
    __syncthreads();
    __syncthreads();
    if (ct < 192) {
#pragma unroll
      for (int r = 0; r < 8; ++r) gq[r] = __hip_atomic_load(ws + WS_H1 + (h * 8 + r) * 192 + ct, RLX_AGENT);
    }
    __syncthreads();
    if (ct < 192) {
      for (unsigned spins = 0; spins < SPIN_CAP; ++spins) {
        bool ok = true;
#pragma unroll
        for (int r = 0; r < 8; ++r) ok &= chk_g(gq[r], tag);
        if (__all(ok)) break;
        if (w == 8) STAMP(13);
#pragma unroll
        for (int r = 0; r < 8; ++r) gq[r] = __hip_atomic_load(ws + WS_H1 + (h * 8 + r) * 192 + ct, RLX_AGENT);
      }
      if (w == 8) STAMP(12);
      if (w == 9) STAMP(14);
      if (w == 10) STAMP(15);
      float q = 0.f;
#pragma unroll
      for (int r = 0; r < 8; ++r) q += val_g(gq[r]);
      qs[ct] = (uint16_t)(cvtpk(q, q) & 0xffffu);
    }
    __syncthreads();
    __syncthreads();
    if (w == 8) STAMP(19);
    __syncthreads();
    if (w == 8) STAMP(20);
    {
      gu64* dst = ws + WS_H2 + (h * 8 + s) * 200;
      if (ct < 192) {
        float ov[8];
#pragma unroll
        for (int r = 0; r < 8; ++r) ov[r] = op[r * 192 + ct];
        put_g(dst + ct, tag, ((ov[0] + ov[1]) + (ov[2] + ov[3])) + ((ov[4] + ov[5]) + (ov[6] + ov[7])));
      } else if (ct < 194) {
        const f32x4 m0 = *reinterpret_cast<const f32x4*>(mw), m1 = *reinterpret_cast<const f32x4*>(mw + 4);
        const f32x4 l0 = *reinterpret_cast<const f32x4*>(lw), l1 = *reinterpret_cast<const f32x4*>(lw + 4);
        const float M = fmaxf(fmaxf(fmaxf(m0[0], m0[1]), fmaxf(m0[2], m0[3])), fmaxf(fmaxf(m1[0], m1[1]), fmaxf(m1[2], m1[3])));
        const float L = ((l0[0] + l0[1]) + (l0[2] + l0[3])) + ((l1[0] + l1[1]) + (l1[2] + l1[3]));
        put_g(dst + ct, tag, ct == 192 ? M : L);
      }
    }
    if (w == 8) STAMP(10);
    __syncthreads();
    __syncthreads();
    if (ct < 192) {
      u64 go[8], gs;
      const int sr = (lane >> 1) & 7, sw = lane & 1;
      for (unsigned spins = 0; spins < SPIN_CAP; ++spins) {
        bool ok = true;
        gs = __hip_atomic_load(ws + WS_H2 + (h * 8 + sr) * 200 + 192 + sw, RLX_AGENT); ok &= chk_g(gs, tag);
#pragma unroll
        for (int r = 0; r < 8; ++r) { go[r] = __hip_atomic_load(ws + WS_H2 + (h * 8 + r) * 200 + ct, RLX_AGENT); ok &= chk_g(go[r], tag); }
        if (__all(ok)) break;
        __builtin_amdgcn_s_sleep(1);
      }
      if (w == 8) STAMP(11);
      const float sv = val_g(gs);
      float mr[8], lr[8];
#pragma unroll
      for (int r = 0; r < 8; ++r) { mr[r] = __builtin_bit_cast(float, __builtin_amdgcn_readlane(__builtin_bit_cast(int, sv), 2 * r)); lr[r] = __builtin_bit_cast(float, __builtin_amdgcn_readlane(__builtin_bit_cast(int, sv), 2 * r + 1)); }
      float M = mr[0];
#pragma unroll
      for (int r = 1; r < 8; ++r) M = fmaxf(M, mr[r]);
      float L = 0.f, o = 0.f;
#pragma unroll
      for (int r = 0; r < 8; ++r) { const float f = __builtin_amdgcn_exp2f((mr[r] - M) * LOG2E); L = fmaf(f, lr[r], L); o = fmaf(f, val_g(go[r]), o); }
      os[ct] = bfround(o / L);
    }
    __syncthreads();
    __syncthreads();
    if (ct < 80) {
      float rv[48];
#pragma unroll
      for (int r = 0; r < 48; ++r) rv[r] = red[r * 80 + ct];
      float a0 = 0.f, a1 = 0.f, a2 = 0.f, a3 = 0.f;
#pragma unroll
      for (int r = 0; r < 48; r += 4) { a0 += rv[r]; a1 += rv[r + 1]; a2 += rv[r + 2]; a3 += rv[r + 3]; }
      put_g(ws + WS_H3 + (s * 32 + h) * 80 + ct, tag, (a0 + a1) + (a2 + a3));
    }
    if (w == 8) STAMP(8);
    if (h < 10 && w == 11) {
      const int r0 = lane >> 3, col = lane & 7;
      u64 gf[4];
      for (unsigned spins = 0; spins < SPIN_CAP; ++spins) {
        bool ok = true;
#pragma unroll
        for (int k = 0; k < 4; ++k) { gf[k] = __hip_atomic_load(ws + WS_H3 + (s * 32 + r0 + 8 * k) * 80 + h * 8 + col, RLX_AGENT); ok &= chk_g(gf[k], tag); }
        if (__all(ok)) break;
        __builtin_amdgcn_s_sleep(1);
      }
      float a = (val_g(gf[0]) + val_g(gf[1])) + (val_g(gf[2]) + val_g(gf[3]));
      a += dpp<XOR8>(a);
      a = x16_sum(a);
      if (lane < 8) out[s * 80 + h * 8 + lane] = (uint16_t)(cvtpk(a, a) & 0xffffu);
      STAMP(9);
      if (b == 0 && lane == 0) __hip_atomic_store(ws + WS_EPOCH, ep1, RLX_AGENT);
    }
  }
  STAMP_DUMP;
}

extern "C" void kernel_launch(void* const* d_in, const int* in_sizes, int n_in, void* d_out, int out_size, void* d_ws, size_t ws_size, hipStream_t stream) {
  fused<<<256, 768, 0, stream>>>((const uint16_t*)d_in[0], (const uint16_t*)d_in[1], (const uint16_t*)d_in[2], (const uint16_t*)d_in[3], (const uint16_t*)d_in[4],
                                 (uint16_t*)d_out, (u64*)d_ws);
}
```

```cpp
#include <hip/hip_runtime.h>
#include <hip/hip_bf16.h>
#include <stdint.h>

typedef unsigned u32x4 __attribute__((ext_vector_type(4)));
typedef float f32x4 __attribute__((ext_vector_type(4)));
typedef __bf16 bf16x2_t __attribute__((ext_vector_type(2)));
typedef unsigned long long u64;
typedef __attribute__((address_space(1))) u64 gu64;

constexpr int DM = 640, NH = 32, SK = 4096, DH = 192, WIN = 1024, NS = 8;
constexpr int HD = NH * DH;
constexpr int CODEPF = 64;
constexpr int NKE = 5;
constexpr int NV1 = 3;
constexpr unsigned SPIN_CAP = 1u << 18;

constexpr int WS_EPOCH = 0;
constexpr int WS_H1 = 64;
constexpr int WS_H2 = WS_H1 + NH * NS * 192;
constexpr int WS_H3 = WS_H2 + NH * NS * 200;

__device__ __forceinline__ float bflo(unsigned u) { return __uint_as_float(u << 16); }
__device__ __forceinline__ float bfhi(unsigned u) { return __uint_as_float(u & 0xffff0000u); }
__device__ __forceinline__ float dot2(unsigned k, unsigned q, float acc) {
  return __builtin_amdgcn_fdot2_f32_bf16(__builtin_bit_cast(bf16x2_t, k), __builtin_bit_cast(bf16x2_t, q), acc, false);
}
__device__ __forceinline__ unsigned cvtpk(float lo, float hi) {
  unsigned r; asm volatile("v_cvt_pk_bf16_f32 %0, %1, %2" : "=v"(r) : "v"(lo), "v"(hi)); return r;
}
__device__ __forceinline__ float bfround(float x) { return bflo(cvtpk(x, x)); }
template <int CTRL> __device__ __forceinline__ float dpp(float x) {
  return __builtin_bit_cast(float, __builtin_amdgcn_mov_dpp(__builtin_bit_cast(int, x), CTRL, 0xf, 0xf, true));
}
constexpr int XOR1 = 0xB1, XOR2 = 0x4E, XOR7 = 0x141, XOR8 = 0x128;
__device__ __forceinline__ u32x4 ldnt(const void* p) { return __builtin_nontemporal_load(reinterpret_cast<const u32x4*>(p)); }
__device__ __forceinline__ float x16_sum(float l) {
  { auto r = __builtin_amdgcn_permlane16_swap(__float_as_uint(l), __float_as_uint(l), false, false); l = __uint_as_float(r[0]) + __uint_as_float(r[1]); }
  { auto r = __builtin_amdgcn_permlane32_swap(__float_as_uint(l), __float_as_uint(l), false, false); l = __uint_as_float(r[0]) + __uint_as_float(r[1]); }
  return l;
}
__device__ __forceinline__ float x16_max(float m) {
  { auto r = __builtin_amdgcn_permlane16_swap(__float_as_uint(m), __float_as_uint(m), false, false); m = fmaxf(__uint_as_float(r[0]), __uint_as_float(r[1])); }
  { auto r = __builtin_amdgcn_permlane32_swap(__float_as_uint(m), __float_as_uint(m), false, false); m = fmaxf(__uint_as_float(r[0]), __uint_as_float(r[1])); }
  return m;
}
#define RLX_AGENT __ATOMIC_RELAXED, __HIP_MEMORY_SCOPE_AGENT
__device__ __forceinline__ void put_g(gu64* g, u64 tag, float v) {
  const unsigned hi = ((__float_as_uint(v) + 0x80u) & 0xffffff00u) | (unsigned)(tag >> 32);
  __hip_atomic_store(g, ((u64)hi << 32) | (unsigned)tag, RLX_AGENT);
}
__device__ __forceinline__ bool chk_g(u64 x, u64 tag) { return (x & 0xffffffffffull) == tag; }
__device__ __forceinline__ float val_g(u64 x) { return __uint_as_float((unsigned)(x >> 32) & 0xffffff00u); }

constexpr float LOG2E = 1.4426950408889634f;
#define SB() __builtin_amdgcn_sched_barrier(0)

__global__ void __launch_bounds__(768) fused(const uint16_t* __restrict__ x, const uint16_t* __restrict__ kc, const uint16_t* __restrict__ vc,
                                             const uint16_t* __restrict__ wq, const uint16_t* __restrict__ wo, uint16_t* __restrict__ out, u64* ws_) {
  __shared__ __attribute__((aligned(16))) float red[48 * 80 + 32 * 8];
  __shared__ __attribute__((aligned(16))) float op[8 * 192];
  __shared__ __attribute__((aligned(16))) uint16_t qs[192];
  __shared__ __attribute__((aligned(16))) float os[192];
  __shared__ __attribute__((aligned(16))) float mw[8];
  __shared__ __attribute__((aligned(16))) float lw[8];
  u64 pc0; asm volatile("s_getpc_b64 %0" : "=s"(pc0));
  gu64* ws = (gu64*)ws_;
  const int b = blockIdx.x, jj = b >> 3;
  const int h = (b & 7) * 4 + (jj >> 3), s = jj & 7;
  const int t = threadIdx.x, w = t >> 6, lane = t & 63;
  if (w < 8) {
    const int tc = t < 480 ? t : t - 32;
    const int cA = tc % 24, rgA = tc / 24, cD = tc % 10, rgD = tc / 10;
    const int g = lane >> 3, c = lane & 7;
    u32x4 wa[4], K[2][3], V[2][3], wd[4]; uint16_t xr[4];
    const bool act = t < 480;
#pragma unroll
    for (int j = 0; j < 4; ++j) xr[j] = x[s * 80 + rgA + 20 * j];
#pragma unroll
    for (int j = 0; j < 4; ++j) wa[j] = ldnt(wq + (size_t)(s * 80 + rgA + 20 * j) * HD + h * DH + cA * 8);
    SB();
    __syncthreads();
    const size_t base = ((size_t)h * SK + (SK - WIN) + s * 128 + w * 16 + g) * DH + c * 8;
#pragma unroll
    for (int i = 0; i < NKE; ++i) K[i / 3][i % 3] = ldnt(kc + base + (i / 3) * 8 * DH + (i % 3) * 64);
    SB();
    {
      float acc[8];
#pragma unroll
      for (int e = 0; e < 8; ++e) acc[e] = 0.f;
#pragma unroll
      for (int j = 0; j < 4; ++j) { const float xv = bflo(xr[j]);
#pragma unroll
        for (int e = 0; e < 4; ++e) { acc[2 * e] = fmaf(xv, bflo(wa[j][e]), acc[2 * e]); acc[2 * e + 1] = fmaf(xv, bfhi(wa[j][e]), acc[2 * e + 1]); } }
      float* dst = red + (act ? rgA * 192 + cA * 8 : 48 * 80 + (t - 480) * 8);
      *reinterpret_cast<f32x4*>(dst) = f32x4{acc[0], acc[1], acc[2], acc[3]};
      *reinterpret_cast<f32x4*>(dst + 4) = f32x4{acc[4], acc[5], acc[6], acc[7]};
    }
    __syncthreads();
    __syncthreads();
#pragma unroll
    for (int i = NKE; i < 6; ++i) K[i / 3][i % 3] = ldnt(kc + base + (i / 3) * 8 * DH + (i % 3) * 64);
#pragma unroll
    for (int i = 0; i < NV1; ++i) V[i / 3][i % 3] = ldnt(vc + base + (i / 3) * 8 * DH + (i % 3) * 64);
    SB();
    __syncthreads();
    __syncthreads();
#pragma unroll
    for (int i = NV1; i < 6; ++i) V[i / 3][i % 3] = ldnt(vc + base + (i / 3) * 8 * DH + (i % 3) * 64);
    SB();
    __syncthreads();
    u32x4 qf[3];
#pragma unroll
    for (int u = 0; u < 3; ++u) qf[u] = *reinterpret_cast<const u32x4*>(qs + u * 64 + c * 8);
    float sc[2];
#pragma unroll
    for (int j = 0; j < 2; ++j) {
      float d0 = 0.f, d1 = 0.f, d2 = 0.f;
#pragma unroll
      for (int e = 0; e < 4; ++e) { d0 = dot2(K[j][0][e], qf[0][e], d0); d1 = dot2(K[j][1][e], qf[1][e], d1); d2 = dot2(K[j][2][e], qf[2][e], d2); }
      float d = (d0 + d1) + d2;
      d += dpp<XOR1>(d); d += dpp<XOR2>(d); d += dpp<XOR7>(d);
      sc[j] = bfround(bfround(d) * 0.07216878364870322f);
    }
    float m = fmaxf(sc[0], sc[1]);
    m = fmaxf(m, dpp<XOR8>(m));
    m = x16_max(m);
    if (lane == 0) mw[w] = m;
    __syncthreads();
    {
      const f32x4 m0 = *reinterpret_cast<const f32x4*>(mw), m1 = *reinterpret_cast<const f32x4*>(mw + 4);
      m = fmaxf(fmaxf(fmaxf(m0[0], m0[1]), fmaxf(m0[2], m0[3])), fmaxf(fmaxf(m1[0], m1[1]), fmaxf(m1[2], m1[3])));
    }
    const float p0 = __builtin_amdgcn_exp2f((sc[0] - m) * LOG2E), p1 = __builtin_amdgcn_exp2f((sc[1] - m) * LOG2E);
    float l = p0 + p1;
    l += dpp<XOR8>(l);
    l = x16_sum(l);
    if (lane == 0) lw[w] = l;
    SB();
#pragma unroll
    for (int j = 0; j < 4; ++j) wd[j] = ldnt(wo + (size_t)(h * DH + rgD + 48 * j) * DM + s * 80 + cD * 8);
    SB();
    float acc[24];
#pragma unroll
    for (int u = 0; u < 3; ++u)
#pragma unroll
      for (int e = 0; e < 4; ++e) {
        acc[u * 8 + 2 * e] = fmaf(p1, bflo(V[1][u][e]), p0 * bflo(V[0][u][e]));
        acc[u * 8 + 2 * e + 1] = fmaf(p1, bfhi(V[1][u][e]), p0 * bfhi(V[0][u][e]));
      }
    float a12[12], a6[6], a3[3];
#pragma unroll
    for (int a = 0; a < 12; ++a) { auto r = __builtin_amdgcn_permlane32_swap(__float_as_uint(acc[a]), __float_as_uint(acc[a + 12]), false, false); a12[a] = __uint_as_float(r[0]) + __uint_as_float(r[1]); }
#pragma unroll
    for (int a = 0; a < 6; ++a) { auto r = __builtin_amdgcn_permlane16_swap(__float_as_uint(a12[a]), __float_as_uint(a12[a + 6]), false, false); a6[a] = __uint_as_float(r[0]) + __uint_as_float(r[1]); }
#pragma unroll
    for (int a = 0; a < 3; ++a) { const float t1 = a6[a] + dpp<XOR8>(a6[a]), t2 = a6[a + 3] + dpp<XOR8>(a6[a + 3]); a3[a] = (lane & 8) ? t2 : t1; }
    const int ib = ((lane >> 5) & 1) * 12 + ((lane >> 4) & 1) * 6 + ((lane >> 3) & 1) * 3;
#pragma unroll
    for (int a = 0; a < 3; ++a) { const int idx = ib + a; op[w * 192 + (idx >> 3) * 64 + c * 8 + (idx & 7)] = a3[a]; }
    __syncthreads();
    __syncthreads();
    __syncthreads();
    {
      float ac[8];
#pragma unroll
      for (int e = 0; e < 8; ++e) ac[e] = 0.f;
#pragma unroll
      for (int j = 0; j < 4; ++j) { const float ov = os[rgD + 48 * j];
#pragma unroll
        for (int e = 0; e < 4; ++e) { ac[2 * e] = fmaf(ov, bflo(wd[j][e]), ac[2 * e]); ac[2 * e + 1] = fmaf(ov, bfhi(wd[j][e]), ac[2 * e + 1]); } }
      float* dst = red + (act ? rgD * 80 + cD * 8 : 48 * 80 + (t - 480) * 8);
      *reinterpret_cast<f32x4*>(dst) = f32x4{ac[0], ac[1], ac[2], ac[3]};
      *reinterpret_cast<f32x4*>(dst + 4) = f32x4{ac[4], ac[5], ac[6], ac[7]};
    }
    __syncthreads();
  } else {
    __builtin_amdgcn_s_setprio(3);
    const int ct = t - 512;
    u32x4 pfv[12];
#pragma unroll
    for (int i = 0; i < 12; ++i) pfv[i] = u32x4{0u, 0u, 0u, 0u};
    if (w == 11 && b < CODEPF) {
      const char* cb = (const char*)(pc0 & ~63ull);
#pragma unroll
      for (int i = 0; i < 12; ++i) pfv[i] = *reinterpret_cast<const __attribute__((address_space(1))) u32x4*>((const __attribute__((address_space(1))) char*)cb + i * 1024 + lane * 16);
    }
    u64 ep0 = __hip_atomic_load(ws + WS_EPOCH, RLX_AGENT);
    __syncthreads();
    __syncthreads();
    asm volatile("" : "+v"(ep0));
#pragma unroll
    for (int i = 0; i < 12; ++i) asm volatile("" :: "v"(pfv[i]));
    const u64 ep1 = ep0 + 1ull, tag = ep1 & 0xffffffffffull;
    u64 gq[8];
    if (ct < 192) {
      float rv[20];
#pragma unroll
      for (int r = 0; r < 20; ++r) rv[r] = red[r * 192 + ct];
      float a0 = 0.f, a1 = 0.f, a2 = 0.f, a3 = 0.f;
#pragma unroll
      for (int r = 0; r < 20; r += 4) { a0 += rv[r]; a1 += rv[r + 1]; a2 += rv[r + 2]; a3 += rv[r + 3]; }
      put_g(ws + WS_H1 + (h * 8 + s) * 192 + ct, tag, (a0 + a1) + (a2 + a3));
    }
    __syncthreads();
    __syncthreads();
    if (ct < 192) {
#pragma unroll
      for (int r = 0; r < 8; ++r) gq[r] = __hip_atomic_load(ws + WS_H1 + (h * 8 + r) * 192 + ct, RLX_AGENT);
    }
    __syncthreads();
    if (ct < 192) {
      for (unsigned spins = 0; spins < SPIN_CAP; ++spins) {
        bool ok = true;
#pragma unroll
        for (int r = 0; r < 8; ++r) ok &= chk_g(gq[r], tag);
        if (__all(ok)) break;
#pragma unroll
        for (int r = 0; r < 8; ++r) gq[r] = __hip_atomic_load(ws + WS_H1 + (h * 8 + r) * 192 + ct, RLX_AGENT);
      }
      float q = 0.f;
#pragma unroll
      for (int r = 0; r < 8; ++r) q += val_g(gq[r]);
      qs[ct] = (uint16_t)(cvtpk(q, q) & 0xffffu);
    }
    __syncthreads();
    __syncthreads();
    __syncthreads();
    {
      gu64* dst = ws + WS_H2 + (h * 8 + s) * 200;
      if (ct < 192) {
        float ov[8];
#pragma unroll
        for (int r = 0; r < 8; ++r) ov[r] = op[r * 192 + ct];
        put_g(dst + ct, tag, ((ov[0] + ov[1]) + (ov[2] + ov[3])) + ((ov[4] + ov[5]) + (ov[6] + ov[7])));
      } else if (ct < 194) {
        const f32x4 m0 = *reinterpret_cast<const f32x4*>(mw), m1 = *reinterpret_cast<const f32x4*>(mw + 4);
        const f32x4 l0 = *reinterpret_cast<const f32x4*>(lw), l1 = *reinterpret_cast<const f32x4*>(lw + 4);
        const float M = fmaxf(fmaxf(fmaxf(m0[0], m0[1]), fmaxf(m0[2], m0[3])), fmaxf(fmaxf(m1[0], m1[1]), fmaxf(m1[2], m1[3])));
        const float L = ((l0[0] + l0[1]) + (l0[2] + l0[3])) + ((l1[0] + l1[1]) + (l1[2] + l1[3]));
        put_g(dst + ct, tag, ct == 192 ? M : L);
      }
    }
    __syncthreads();
    if (ct < 192) {
      u64 go[8], gs;
      const int sr = (lane >> 1) & 7, sw = lane & 1;
      for (unsigned spins = 0; spins < SPIN_CAP; ++spins) {
        bool ok = true;
        gs = __hip_atomic_load(ws + WS_H2 + (h * 8 + sr) * 200 + 192 + sw, RLX_AGENT); ok &= chk_g(gs, tag);
#pragma unroll
        for (int r = 0; r < 8; ++r) { go[r] = __hip_atomic_load(ws + WS_H2 + (h * 8 + r) * 200 + ct, RLX_AGENT); ok &= chk_g(go[r], tag); }
        if (__all(ok)) break;
        __builtin_amdgcn_s_sleep(1);
      }
      const float sv = val_g(gs);
      float mr[8], lr[8];
#pragma unroll
      for (int r = 0; r < 8; ++r) { mr[r] = __builtin_bit_cast(float, __builtin_amdgcn_readlane(__builtin_bit_cast(int, sv), 2 * r)); lr[r] = __builtin_bit_cast(float, __builtin_amdgcn_readlane(__builtin_bit_cast(int, sv), 2 * r + 1)); }
      float M = mr[0];
#pragma unroll
      for (int r = 1; r < 8; ++r) M = fmaxf(M, mr[r]);
      float L = 0.f, o = 0.f;
#pragma unroll
      for (int r = 0; r < 8; ++r) { const float f = __builtin_amdgcn_exp2f((mr[r] - M) * LOG2E); L = fmaf(f, lr[r], L); o = fmaf(f, val_g(go[r]), o); }
      os[ct] = bfround(o / L);
    }
    __syncthreads();
    __syncthreads();
    if (ct < 80) {
      float rv[48];
#pragma unroll
      for (int r = 0; r < 48; ++r) rv[r] = red[r * 80 + ct];
      float a0 = 0.f, a1 = 0.f, a2 = 0.f, a3 = 0.f;
#pragma unroll
      for (int r = 0; r < 48; r += 4) { a0 += rv[r]; a1 += rv[r + 1]; a2 += rv[r + 2]; a3 += rv[r + 3]; }
      put_g(ws + WS_H3 + (s * 32 + h) * 80 + ct, tag, (a0 + a1) + (a2 + a3));
    }
    if (h < 10 && w == 11) {
      const int r0 = lane >> 3, col = lane & 7;
      u64 gf[4];
      for (unsigned spins = 0; spins < SPIN_CAP; ++spins) {
        bool ok = true;
#pragma unroll
        for (int k = 0; k < 4; ++k) { gf[k] = __hip_atomic_load(ws + WS_H3 + (s * 32 + r0 + 8 * k) * 80 + h * 8 + col, RLX_AGENT); ok &= chk_g(gf[k], tag); }
        if (__all(ok)) break;
        __builtin_amdgcn_s_sleep(1);
      }
      float a = (val_g(gf[0]) + val_g(gf[1])) + (val_g(gf[2]) + val_g(gf[3]));
      a += dpp<XOR8>(a);
      a = x16_sum(a);
      if (lane < 8) out[s * 80 + h * 8 + lane] = (uint16_t)(cvtpk(a, a) & 0xffffu);
      if (b == 0 && lane == 0) __hip_atomic_store(ws + WS_EPOCH, ep1, RLX_AGENT);
    }
  }
}

extern "C" void kernel_launch(void* const* d_in, const int* in_sizes, int n_in, void* d_out, int out_size, void* d_ws, size_t ws_size, hipStream_t stream) {
  fused<<<256, 768, 0, stream>>>((const uint16_t*)d_in[0], (const uint16_t*)d_in[1], (const uint16_t*)d_in[2], (const uint16_t*)d_in[3], (const uint16_t*)d_in[4],
                                 (uint16_t*)d_out, (u64*)d_ws);
}
```
